```python
import torch

def _softmax(x, axis=-1):
    return torch.softmax(x, dim=axis)
def _gelu(x):
    return torch.nn.functional.gelu(x, approximate="tanh")
def _silu(x):
    return torch.nn.functional.silu(x)
def _sigmoid(x):
    return torch.sigmoid(x)
def _layernorm(x, axis=-1, eps=1e-5):
    m = x.mean(dim=axis, keepdim=True)
    v = x.var(dim=axis, keepdim=True, unbiased=False)
    return (x - m) / torch.sqrt(v + eps)
def _scatter_add(tgt, idx, src, axis=0):
    return tgt.clone().index_add_(axis, idx, src)
def _segment_sum(src, seg, n):
    out = torch.zeros((n,) + src.shape[1:], dtype=src.dtype)
    return out.index_add_(0, seg, src)
def _pad(x, pw):
    flat = tuple(v for pair in reversed(pw) for v in pair)
    return torch.nn.functional.pad(x, flat)

kernel_name = 'concat_heads__sliding_block_cached__mha_block_cached__decode'

def setup_inputs():

    _w = (lambda a: (lambda t: t.to(torch.bfloat16) if t.is_floating_point() else t)(torch.as_tensor(a)))
    def _A_setup():
        B, Sq, D = 2, 1, 512
        Hq, Hkv, Sk, Dh = 32, 8, 16384, 128
        assert Hq % Hkv == 0, (Hq, Hkv)
        return {
            "x": _w((lambda *s: torch.randn(*s, dtype=torch.bfloat16) * 0.8)(B, Sq, D)),
            "k_cache": _w((lambda *s: torch.randn(*s, dtype=torch.bfloat16) * 1.6)(B, Hkv, Sk, Dh)),
            "v_cache": _w((lambda *s: torch.randn(*s, dtype=torch.bfloat16) * 0.2)(B, Hkv, Sk, Dh)),
            "wq": _w((lambda *s: torch.randn(*s, dtype=torch.bfloat16) * 1.6)(D, Hq * Dh) * (D ** -0.5)),
            "wo": _w((lambda *s: torch.randn(*s, dtype=torch.bfloat16) * 0.2)(Hq * Dh, D) * ((Hq * Dh) ** -0.5)),
        }
    _A_in = _A_setup()

    def _B_setup():
        B, Sq, D = 2, 1, 512
        H, Sk, Dh = 32, 16384, 128
        return {
            "x": _w((lambda *s: torch.randn(*s, dtype=torch.bfloat16) * 0.8)(B, Sq, D)),
            "k_cache": _w((lambda *s: torch.randn(*s, dtype=torch.bfloat16) * 1.6)(B, H, Sk, Dh)),
            "v_cache": _w((lambda *s: torch.randn(*s, dtype=torch.bfloat16) * 0.2)(B, H, Sk, Dh)),
            "wq": _w((lambda *s: torch.randn(*s, dtype=torch.bfloat16) * 1.6)(D, H * Dh) * (D ** -0.5)),
            "wo": _w((lambda *s: torch.randn(*s, dtype=torch.bfloat16) * 0.2)(H * Dh, D) * ((H * Dh) ** -0.5)),
        }
    _B_in = _B_setup()

    return {
        "A_x": _A_in["x"],
        "A_k_cache": _A_in["k_cache"],
        "A_v_cache": _A_in["v_cache"],
        "A_wq": _A_in["wq"],
        "A_wo": _A_in["wo"],
        "B_x": _B_in["x"],
        "B_k_cache": _B_in["k_cache"],
        "B_v_cache": _B_in["v_cache"],
        "B_wq": _B_in["wq"],
        "B_wo": _B_in["wo"],
    }

def reference(A_x, A_k_cache, A_v_cache, A_wq, A_wo, B_x, B_k_cache, B_v_cache, B_wq, B_wo):
    def _A_ref(x, k_cache, v_cache, wq, wo):
        B, Sq, D = x.shape
        Hq, Dh = 32, 128
        Hkv, Sk = k_cache.shape[1], k_cache.shape[2]
        W = min(4096, max(256, Sk // 4))
        rep = Hq // Hkv
        q = (lambda x, *d: x.permute(*d))((x @ wq).reshape(B, Sq, Hq, Dh), 0, 2, 1, 3)
        k = (lambda x, n, axis: torch.repeat_interleave(x, n, dim=axis))(k_cache[:, :, Sk - W:, :], rep, axis=1)
        v = (lambda x, n, axis: torch.repeat_interleave(x, n, dim=axis))(v_cache[:, :, Sk - W:, :], rep, axis=1)
        scores = torch.einsum("bhqd,bhkd->bhqk", q, k) * (Dh ** -0.5)
        p = _softmax(scores, axis=-1)
        o = torch.einsum("bhqk,bhkd->bhqd", p, v)
        return (lambda x, *d: x.permute(*d))(o, 0, 2, 1, 3).reshape(B, Sq, Hq * Dh) @ wo

    def _B_ref(x, k_cache, v_cache, wq, wo):
        B, Sq, D = x.shape
        H, Dh = 32, 128
        q = (lambda x, *d: x.permute(*d))((x @ wq).reshape(B, Sq, H, Dh), 0, 2, 1, 3)
        scores = torch.einsum("bhqd,bhkd->bhqk", q, k_cache) * (Dh ** -0.5)
        p = _softmax(scores, axis=-1)
        o = torch.einsum("bhqk,bhkd->bhqd", p, v_cache)
        return (lambda x, *d: x.permute(*d))(o, 0, 2, 1, 3).reshape(B, Sq, H * Dh) @ wo

    a = _A_ref(A_x, A_k_cache, A_v_cache, A_wq, A_wo)
    b = _B_ref(B_x, B_k_cache, B_v_cache, B_wq, B_wo)
    return torch.concatenate([a, b], axis=-1)
```

```cpp
#include <hip/hip_runtime.h>
#include <hip/hip_bf16.h>
#include <stdint.h>

typedef unsigned u32x4 __attribute__((ext_vector_type(4)));
typedef float f32x4 __attribute__((ext_vector_type(4)));
typedef float f32x2 __attribute__((ext_vector_type(2)));
typedef __bf16 bf16x2 __attribute__((ext_vector_type(2)));
typedef __amdgpu_buffer_rsrc_t rsrc_t;

#define LPT_B 4
#define AUX_KV 2
#define AUX_W 0

constexpr int SK = 16384, WIN = 4096;
constexpr int NSB = 7;
constexpr int NQA = 4;
constexpr float SCL = 0.08838834764831845f * 1.4426950408889634f;
constexpr float M_INIT = -1e30f;

constexpr int WS_YB = 0;
constexpr int WS_YA = WS_YB + 2 * 32 * NSB * 512;
constexpr int WS_MLB = WS_YA + 2 * 32 * NQA * 512;
constexpr int WS_MLA = WS_MLB + 2 * 32 * 8 * 2;

constexpr int L_QP = 0;
constexpr int L_REC = L_QP + 8192;
constexpr int L_ML = L_REC + 4096;
constexpr int L_OPK = L_ML + 64;
constexpr int L_TOTAL = 24576;

#define PIN() do { asm volatile("" ::: "memory"); __builtin_amdgcn_sched_barrier(0); } while (0)

__device__ __forceinline__ float dot2(unsigned a, unsigned b, float c) {
  return __builtin_amdgcn_fdot2_f32_bf16(__builtin_bit_cast(bf16x2, a), __builtin_bit_cast(bf16x2, b), c, false);
}
__device__ __forceinline__ unsigned pk_lo(unsigned a, unsigned b) { return __builtin_amdgcn_perm(b, a, 0x05040100u); }
__device__ __forceinline__ unsigned pk_hi(unsigned a, unsigned b) { return __builtin_amdgcn_perm(b, a, 0x07060302u); }
__device__ __forceinline__ unsigned cvtpk(float lo, float hi) {
  bf16x2 r; r[0] = (__bf16)lo; r[1] = (__bf16)hi; return __builtin_bit_cast(unsigned, r);
}
template <int CTRL> __device__ __forceinline__ float dppf(float x) {
  return __builtin_bit_cast(float, __builtin_amdgcn_mov_dpp(__builtin_bit_cast(int, x), CTRL, 0xf, 0xf, true));
}
template <int CTRL> __device__ __forceinline__ unsigned dppu(unsigned x) { return (unsigned)__builtin_amdgcn_mov_dpp((int)x, CTRL, 0xf, 0xf, true); }
constexpr int XOR1 = 0xB1, XOR2 = 0x4E, XOR7 = 0x141, ROR8 = 0x128, ROR4 = 0x124, ROR12 = 0x12C, BCAST = 0x150;
__device__ __forceinline__ float row_sum(float x) {
  x += dppf<ROR8>(x); x += dppf<XOR7>(x); x += dppf<XOR2>(x); x += dppf<XOR1>(x); return x;
}
__device__ __forceinline__ float swadd32(float a, float b) {
  auto s = __builtin_amdgcn_permlane32_swap(__float_as_uint(a), __float_as_uint(b), false, false);
  return __uint_as_float(s[0]) + __uint_as_float(s[1]);
}
__device__ __forceinline__ float swadd16(float a, float b) {
  auto s = __builtin_amdgcn_permlane16_swap(__float_as_uint(a), __float_as_uint(b), false, false);
  return __uint_as_float(s[0]) + __uint_as_float(s[1]);
}
__device__ __forceinline__ float xrow_sum(float x) { x = swadd16(x, x); return swadd32(x, x); }
__device__ __forceinline__ float xrow_max(float x) {
  auto s = __builtin_amdgcn_permlane16_swap(__float_as_uint(x), __float_as_uint(x), false, false);
  x = fmaxf(__uint_as_float(s[0]), __uint_as_float(s[1]));
  auto t = __builtin_amdgcn_permlane32_swap(__float_as_uint(x), __float_as_uint(x), false, false);
  return fmaxf(__uint_as_float(t[0]), __uint_as_float(t[1]));
}
__device__ __forceinline__ float ex2(float x) { return __builtin_amdgcn_exp2f(x); }
__device__ __forceinline__ rsrc_t mkrs(const void* p, int bytes) { return __builtin_amdgcn_make_buffer_rsrc((void*)p, 0, bytes, 0x00020000); }
template <int AUX> __device__ __forceinline__ u32x4 bld(rsrc_t rs, int vo, int so) { return __builtin_amdgcn_raw_buffer_load_b128(rs, vo, so, AUX); }

__device__ __forceinline__ void gemv16(const u32x4 (&W)[16], const u32x4 (&x0)[2], const u32x4 (&x1)[2], float (&a)[16]) {
#pragma unroll
  for (int k = 0; k < 16; ++k) a[k] = 0.f;
#pragma unroll
  for (int k = 0; k < 8; ++k) {
    const unsigned p0 = x0[k >> 2][k & 3], p1 = x1[k >> 2][k & 3];
#pragma unroll
    for (int j = 0; j < 4; ++j) {
      const unsigned lo = pk_lo(W[2 * k][j], W[2 * k + 1][j]), hi = pk_hi(W[2 * k][j], W[2 * k + 1][j]);
      a[2 * j] = dot2(lo, p0, a[2 * j]); a[2 * j + 1] = dot2(hi, p0, a[2 * j + 1]);
      a[8 + 2 * j] = dot2(lo, p1, a[8 + 2 * j]); a[8 + 2 * j + 1] = dot2(hi, p1, a[8 + 2 * j + 1]);
    }
  }
}
__device__ __forceinline__ f32x4 red54(const float (&a)[16]) {
  float t[8];
#pragma unroll
  for (int i = 0; i < 8; ++i) t[i] = swadd32(a[i], a[i + 8]);
  f32x4 u;
#pragma unroll
  for (int i = 0; i < 4; ++i) u[i] = swadd16(t[i], t[i + 4]);
  return u;
}
__device__ __forceinline__ void wq_issue(u32x4 (&W)[16], rsrc_t rs, int vo, int so) {
#pragma unroll
  for (int i = 0; i < 16; ++i) W[i] = bld<AUX_W>(rs, vo, so + i * 8192);
}
__device__ __forceinline__ void wo_issue(u32x4 (&W)[16], rsrc_t rs, int vo, int so) {
#pragma unroll
  for (int i = 0; i < 16; ++i) W[i] = bld<AUX_W>(rs, vo, so + i * 1024);
}
__device__ __forceinline__ u32x4 q_gather(const float* qp, int b, int r, int c) {
  const float* p = qp + (2 * r) * 256 + b * 128 + 8 * c;
  const f32x4 a0 = *(const f32x4*)p, a1 = *(const f32x4*)(p + 4), b0 = *(const f32x4*)(p + 256), b1 = *(const f32x4*)(p + 260);
  float v[8];
#pragma unroll
  for (int i = 0; i < 4; ++i) { v[i] = xrow_sum(a0[i] + b0[i]); v[4 + i] = xrow_sum(a1[i] + b1[i]); }
  return u32x4{cvtpk(v[0], v[1]), cvtpk(v[2], v[3]), cvtpk(v[4], v[5]), cvtpk(v[6], v[7])};
}
__device__ __forceinline__ void oproj_store(const u32x4 (&W)[16], const unsigned* opk0, const unsigned* opk1, float* y0, float* y1, int w, int lane) {
  const int rr = lane >> 3;
  u32x4 o0[2], o1[2];
  o0[0] = *(const u32x4*)(opk0 + 8 * rr); o0[1] = *(const u32x4*)(opk0 + 8 * rr + 4);
  o1[0] = *(const u32x4*)(opk1 + 8 * rr); o1[1] = *(const u32x4*)(opk1 + 8 * rr + 4);
  float a[16];
  gemv16(W, o0, o1, a);
  f32x4 u = red54(a);
#pragma unroll
  for (int i = 0; i < 4; ++i) u[i] += dppf<ROR8>(u[i]);
  float* y = (lane >> 5) ? y1 : y0;
  if (!(lane & 8)) *(f32x4*)(y + 64 * w + 8 * (lane & 7) + 4 * ((lane >> 4) & 1)) = u;
}

__global__ void __launch_bounds__(512, 2)
attn_main(const unsigned short* Ax, const unsigned short* Ak, const unsigned short* Av, const unsigned short* Awq, const unsigned short* Awo,
          const unsigned short* Bx, const unsigned short* Bk, const unsigned short* Bv, const unsigned short* Bwq, const unsigned short* Bwo, float* ws) {
  __shared__ __attribute__((aligned(16))) float lds[L_TOTAL];
  const int bid = blockIdx.x, tid = threadIdx.x, lane = tid & 63;
  const int w = __builtin_amdgcn_readfirstlane(tid >> 6);
  const int r = lane >> 4, c = lane & 15;
  const int xcd = bid & 7, j = bid >> 3;
  const int b = w >> 2, wb = w & 3;
  const int vo_kv = lane * 16;
  const int vo_wq = r * (16 * 8192) + c * 16;
  const int vo_wo = (lane >> 3) * (16 * 1024) + (lane & 7) * 16;
  unsigned* opk = (unsigned*)(lds + L_OPK);

  if (j < 4 * NSB) {
    constexpr int LPT = LPT_B, TK = 4 * LPT, TB = TK * 256, NT = SK / TK, G = NSB * 4;
    const int h = xcd + 8 * (j / NSB), s = j % NSB, g = s * 4 + wb;
    const rsrc_t rx = mkrs(Bx, 2048), rwq = mkrs(Bwq, 512 * 4096 * 2), rwo = mkrs(Bwo, 512 * 4096 * 2);
    const size_t kvbase = (size_t)(b * 32 + h) * SK * 128;
    const rsrc_t rk = mkrs(Bk + kvbase, SK * 256), rv = mkrs(Bv + kvbase, SK * 256);
    u32x4 x0[2], x1[2], W[16], K[LPT], V[LPT];
    x0[0] = bld<0>(rx, r * 32, w * 128); x0[1] = bld<0>(rx, r * 32 + 16, w * 128);
    x1[0] = bld<0>(rx, r * 32, 1024 + w * 128); x1[1] = bld<0>(rx, r * 32 + 16, 1024 + w * 128);
    wq_issue(W, rwq, vo_wq, w * (64 * 8192) + h * 256);
    PIN();
    int so = g * TB;
#pragma unroll
    for (int i = 0; i < LPT; ++i) K[i] = bld<AUX_KV>(rk, vo_kv + i * 1024, so);
    PIN();
#pragma unroll
    for (int i = 0; i < LPT; ++i) V[i] = bld<AUX_KV>(rv, vo_kv + i * 1024, so);
    PIN();
    {
      float a[16];
      gemv16(W, x0, x1, a);
      const f32x4 u = red54(a);
      *(f32x4*)(lds + L_QP + w * 256 + (r >> 1) * 128 + 8 * c + 4 * (r & 1)) = u;
    }
    PIN();
    wo_issue(W, rwo, vo_wo, h * (128 * 1024) + w * 128);
    PIN();
    __syncthreads();
    const u32x4 q = q_gather(lds + L_QP, b, r, c);
    float m = M_INIT, l = 0.f, acc[8];
#pragma unroll
    for (int k = 0; k < 8; ++k) acc[k] = 0.f;
    const int nt = (NT - g + G - 1) / G;
    for (int t = 0; t < nt; ++t) {
      so += G * TB;
      float sc[LPT];
#pragma unroll
      for (int i = 0; i < LPT; ++i) {
        float d = dot2(K[i][0], q[0], 0.f);
        d = dot2(K[i][1], q[1], d); d = dot2(K[i][2], q[2], d); sc[i] = dot2(K[i][3], q[3], d);
      }
#pragma unroll
      for (int i = 0; i < LPT; ++i) asm volatile("" : "+v"(sc[i]) :: "memory");
      __builtin_amdgcn_sched_barrier(0);
#pragma unroll
      for (int i = 0; i < LPT; ++i) K[i] = bld<AUX_KV>(rk, vo_kv + i * 1024, so);
      PIN();
      float mx = M_INIT;
#pragma unroll
      for (int i = 0; i < LPT; ++i) { sc[i] = row_sum(sc[i]); mx = fmaxf(mx, sc[i]); }
      if (__any(mx > m)) {
        const float mn = fmaxf(m, mx), al = ex2((m - mn) * SCL);
        m = mn; l *= al;
#pragma unroll
        for (int k = 0; k < 8; ++k) acc[k] *= al;
      }
      const float mc = m * SCL;
      float p[LPT];
#pragma unroll
      for (int i = 0; i < LPT; ++i) { p[i] = ex2(fmaf(sc[i], SCL, -mc)); l += p[i]; }
#pragma unroll
      for (int i = 0; i < LPT; i += 2) {
        const unsigned pp = cvtpk(p[i], p[i + 1]);
#pragma unroll
        for (int jj = 0; jj < 4; ++jj) {
          acc[2 * jj] = dot2(pk_lo(V[i][jj], V[i + 1][jj]), pp, acc[2 * jj]);
          acc[2 * jj + 1] = dot2(pk_hi(V[i][jj], V[i + 1][jj]), pp, acc[2 * jj + 1]);
        }
      }
#pragma unroll
      for (int k = 0; k < 8; ++k) asm volatile("" : "+v"(acc[k]) :: "memory");
      __builtin_amdgcn_sched_barrier(0);
#pragma unroll
      for (int i = 0; i < LPT; ++i) V[i] = bld<AUX_KV>(rv, vo_kv + i * 1024, so);
      PIN();
    }
    {
      const float mm = xrow_max(m), sc = ex2((m - mm) * SCL);
      l = xrow_sum(l * sc);
      float t[4];
#pragma unroll
      for (int i = 0; i < 4; ++i) t[i] = swadd32(acc[i] * sc, acc[i + 4] * sc);
      f32x2 u; u[0] = swadd16(t[0], t[2]); u[1] = swadd16(t[1], t[3]);
      *(f32x2*)(lds + L_REC + w * 512 + 8 * c + 4 * (r >> 1) + 2 * (r & 1)) = u;
      if (lane == 0) *(f32x2*)(lds + L_ML + w * 8) = f32x2{mm, l};
    }
    __syncthreads();
    if (wb == 0) {
      f32x2 ml[4];
#pragma unroll
      for (int i = 0; i < 4; ++i) ml[i] = *(const f32x2*)(lds + L_ML + (w + i) * 8);
      const float M = fmaxf(fmaxf(ml[0][0], ml[1][0]), fmaxf(ml[2][0], ml[3][0]));
      float L = 0.f, o0 = 0.f, o1 = 0.f;
#pragma unroll
      for (int i = 0; i < 4; ++i) {
        const float e = ex2((ml[i][0] - M) * SCL);
        const f32x2 v = *(const f32x2*)(lds + L_REC + (w + i) * 512 + 2 * lane);
        L = fmaf(e, ml[i][1], L); o0 = fmaf(e, v[0], o0); o1 = fmaf(e, v[1], o1);
      }
      opk[b * 256 + lane] = cvtpk(o0, o1);
      if (lane == 0) *(f32x2*)(ws + WS_MLB + ((b * 32 + h) * 8 + s) * 2) = f32x2{M, L};
    }
    __syncthreads();
    oproj_store(W, opk, opk + 256, ws + WS_YB + (h * NSB + s) * 512, ws + WS_YB + ((32 + h) * NSB + s) * 512, w, lane);
  } else {
    constexpr int TB = 16 * 256;
    const int kvh = xcd, qt = j - 4 * NSB;
    const rsrc_t rx = mkrs(Ax, 2048), rwq = mkrs(Awq, 512 * 4096 * 2), rwo = mkrs(Awo, 512 * 4096 * 2);
    const size_t kvbase = ((size_t)(b * 8 + kvh) * SK + (SK - WIN) + qt * (WIN / NQA)) * 128;
    const rsrc_t rk = mkrs(Ak + kvbase, (WIN / NQA) * 256), rv = mkrs(Av + kvbase, (WIN / NQA) * 256);
    u32x4 x0[2], x1[2], W0[16], W1[16], K[4], V[4];
    x0[0] = bld<0>(rx, r * 32, w * 128); x0[1] = bld<0>(rx, r * 32 + 16, w * 128);
    x1[0] = bld<0>(rx, r * 32, 1024 + w * 128); x1[1] = bld<0>(rx, r * 32 + 16, 1024 + w * 128);
    const int sq = w * (64 * 8192) + kvh * 1024;
    float* qdst = lds + L_QP + w * 256 + (r >> 1) * 128 + 8 * c + 4 * (r & 1);
    wq_issue(W0, rwq, vo_wq, sq);
    PIN();
    wq_issue(W1, rwq, vo_wq, sq + 256);
    PIN();
    { float a[16]; gemv16(W0, x0, x1, a); *(f32x4*)(qdst) = red54(a); }
    PIN();
    wq_issue(W0, rwq, vo_wq, sq + 512);
    PIN();
    { float a[16]; gemv16(W1, x0, x1, a); *(f32x4*)(qdst + 2048) = red54(a); }
    PIN();
    wq_issue(W1, rwq, vo_wq, sq + 768);
    PIN();
    int so = wb * TB;
#pragma unroll
    for (int i = 0; i < 4; ++i) K[i] = bld<AUX_KV>(rk, vo_kv + i * 1024, so);
    PIN();
#pragma unroll
    for (int i = 0; i < 4; ++i) V[i] = bld<AUX_KV>(rv, vo_kv + i * 1024, so);
    PIN();
    { float a[16]; gemv16(W0, x0, x1, a); *(f32x4*)(qdst + 4096) = red54(a); }
    PIN();
    { float a[16]; gemv16(W1, x0, x1, a); *(f32x4*)(qdst + 6144) = red54(a); }
    PIN();
    const int swo = kvh * (512 * 1024) + w * 128;
    wo_issue(W0, rwo, vo_wo, swo);
    PIN();
    __syncthreads();
    u32x4 q[4];
#pragma unroll
    for (int hh = 0; hh < 4; ++hh) q[hh] = q_gather(lds + L_QP + hh * 2048, b, r, c);
    float m = M_INIT, l = 0.f, acc[32];
#pragma unroll
    for (int k = 0; k < 32; ++k) acc[k] = 0.f;
    constexpr int NTA = WIN / NQA / 16 / 4;
    for (int t = 0; t < NTA; ++t) {
      so += 4 * TB;
      float x[16];
#pragma unroll
      for (int i = 0; i < 4; ++i)
#pragma unroll
        for (int hh = 0; hh < 4; ++hh) {
          float d = dot2(K[i][0], q[hh][0], 0.f);
          d = dot2(K[i][1], q[hh][1], d); d = dot2(K[i][2], q[hh][2], d); x[i * 4 + hh] = dot2(K[i][3], q[hh][3], d);
        }
#pragma unroll
      for (int i = 0; i < 16; ++i) asm volatile("" : "+v"(x[i]) :: "memory");
      __builtin_amdgcn_sched_barrier(0);
#pragma unroll
      for (int i = 0; i < 4; ++i) K[i] = bld<AUX_KV>(rk, vo_kv + i * 1024, so);
      PIN();
#define TR_STEP(HALF, CTRL, BIT) _Pragma("unroll") for (int hh = 0; hh < (HALF); ++hh) { \
        const float lo_ = x[hh], hi_ = x[hh + (HALF)]; const float t1_ = lo_ + dppf<CTRL>(lo_), t2_ = hi_ + dppf<CTRL>(hi_); \
        x[hh] = (c & (BIT)) ? t2_ : t1_; }
      TR_STEP(8, ROR8, 8) TR_STEP(4, XOR7, 4) TR_STEP(2, XOR2, 2) TR_STEP(1, XOR1, 1)
#undef TR_STEP
      const float sc = x[0];
      float mx = fmaxf(sc, dppf<ROR4>(sc)); mx = fmaxf(mx, dppf<ROR8>(mx));
      if (__any(mx > m)) {
        const float mn = fmaxf(m, mx), al = ex2((m - mn) * SCL);
        m = mn; l *= al;
        const float a0 = dppf<BCAST + 0>(al), a1 = dppf<BCAST + 1>(al), a2 = dppf<BCAST + 2>(al), a3 = dppf<BCAST + 3>(al);
#pragma unroll
        for (int k = 0; k < 8; ++k) { acc[k] *= a0; acc[8 + k] *= a1; acc[16 + k] *= a2; acc[24 + k] *= a3; }
      }
      const float p = ex2(fmaf(sc, SCL, -m * SCL));
      l += p;
      const unsigned pp = cvtpk(p, dppf<ROR12>(p));
      unsigned pb[8];
      pb[0] = dppu<BCAST + 0>(pp); pb[1] = dppu<BCAST + 1>(pp); pb[2] = dppu<BCAST + 2>(pp); pb[3] = dppu<BCAST + 3>(pp);
      pb[4] = dppu<BCAST + 8>(pp); pb[5] = dppu<BCAST + 9>(pp); pb[6] = dppu<BCAST + 10>(pp); pb[7] = dppu<BCAST + 11>(pp);
#pragma unroll
      for (int i = 0; i < 2; ++i)
#pragma unroll
        for (int jj = 0; jj < 4; ++jj) {
          const unsigned lo = pk_lo(V[2 * i][jj], V[2 * i + 1][jj]), hi = pk_hi(V[2 * i][jj], V[2 * i + 1][jj]);
#pragma unroll
          for (int hh = 0; hh < 4; ++hh) {
            acc[hh * 8 + 2 * jj] = dot2(lo, pb[i * 4 + hh], acc[hh * 8 + 2 * jj]);
            acc[hh * 8 + 2 * jj + 1] = dot2(hi, pb[i * 4 + hh], acc[hh * 8 + 2 * jj + 1]);
          }
        }
#pragma unroll
      for (int k = 0; k < 32; ++k) asm volatile("" : "+v"(acc[k]) :: "memory");
      __builtin_amdgcn_sched_barrier(0);
#pragma unroll
      for (int i = 0; i < 4; ++i) V[i] = bld<AUX_KV>(rv, vo_kv + i * 1024, so);
      PIN();
    }
    wo_issue(W1, rwo, vo_wo, swo + 128 * 1024);
    PIN();
    {
      const float mm = xrow_max(m), sc = ex2((m - mm) * SCL);
      float ls = l * sc; ls += dppf<ROR4>(ls); ls += dppf<ROR8>(ls); ls = xrow_sum(ls);
      float sh[4];
      sh[0] = dppf<BCAST + 0>(sc); sh[1] = dppf<BCAST + 1>(sc); sh[2] = dppf<BCAST + 2>(sc); sh[3] = dppf<BCAST + 3>(sc);
#pragma unroll
      for (int hh = 0; hh < 4; ++hh) {
        float t[4];
#pragma unroll
        for (int i = 0; i < 4; ++i) t[i] = swadd32(acc[hh * 8 + i] * sh[hh], acc[hh * 8 + i + 4] * sh[hh]);
        f32x2 u; u[0] = swadd16(t[0], t[2]); u[1] = swadd16(t[1], t[3]);
        *(f32x2*)(lds + L_REC + w * 512 + hh * 128 + 8 * c + 4 * (r >> 1) + 2 * (r & 1)) = u;
      }
      if (lane < 4) *(f32x2*)(lds + L_ML + w * 8 + lane * 2) = f32x2{mm, ls};
    }
    __syncthreads();
    {
      const int w0 = b * 4, hh = wb;
      f32x2 ml[4];
#pragma unroll
      for (int i = 0; i < 4; ++i) ml[i] = *(const f32x2*)(lds + L_ML + (w0 + i) * 8 + hh * 2);
      const float M = fmaxf(fmaxf(ml[0][0], ml[1][0]), fmaxf(ml[2][0], ml[3][0]));
      float L = 0.f, o0 = 0.f, o1 = 0.f;
#pragma unroll
      for (int i = 0; i < 4; ++i) {
        const float e = ex2((ml[i][0] - M) * SCL);
        const f32x2 v = *(const f32x2*)(lds + L_REC + (w0 + i) * 512 + hh * 128 + 2 * lane);
        L = fmaf(e, ml[i][1], L); o0 = fmaf(e, v[0], o0); o1 = fmaf(e, v[1], o1);
      }
      opk[b * 256 + hh * 64 + lane] = cvtpk(o0, o1);
      if (lane == 0) *(f32x2*)(ws + WS_MLA + ((b * 32 + kvh * 4 + hh) * NQA + qt) * 2) = f32x2{M, L};
    }
    __syncthreads();
    float* y0 = ws + WS_YA + ((kvh * 4) * NQA + qt) * 512;
    float* y1 = y0 + 32 * NQA * 512;
    oproj_store(W0, opk, opk + 256, y0, y1, w, lane);
    PIN();
    wo_issue(W0, rwo, vo_wo, swo + 2 * 128 * 1024);
    PIN();
    oproj_store(W1, opk + 64, opk + 256 + 64, y0 + NQA * 512, y1 + NQA * 512, w, lane);
    PIN();
    wo_issue(W1, rwo, vo_wo, swo + 3 * 128 * 1024);
    PIN();
    oproj_store(W0, opk + 128, opk + 256 + 128, y0 + 2 * NQA * 512, y1 + 2 * NQA * 512, w, lane);
    PIN();
    oproj_store(W1, opk + 192, opk + 256 + 192, y0 + 3 * NQA * 512, y1 + 3 * NQA * 512, w, lane);
  }
}

template <int NS, int MLS>
__device__ __forceinline__ f32x4 merge_head(const float* y, const float* ml) {
  f32x4 v[NS]; f32x2 e[NS];
#pragma unroll
  for (int s = 0; s < NS; ++s) { v[s] = *(const f32x4*)(y + s * 512); e[s] = *(const f32x2*)(ml + s * 2); }
  float M = e[0][0];
#pragma unroll
  for (int s = 1; s < NS; ++s) M = fmaxf(M, e[s][0]);
  float L = 0.f; f32x4 o = {0.f, 0.f, 0.f, 0.f};
#pragma unroll
  for (int s = 0; s < NS; ++s) { const float wt = ex2((e[s][0] - M) * SCL); L = fmaf(wt, e[s][1], L); o += wt * v[s]; }
  return o * (1.f / L);
}
__global__ void __launch_bounds__(256) attn_final(const float* ws, unsigned short* out) {
  __shared__ __attribute__((aligned(16))) float red[4 * 32];
  const int bid = blockIdx.x, tid = threadIdx.x, lane = tid & 63, w = tid >> 6;
  const int prob = bid >> 5, b = (bid >> 4) & 1, cg = bid & 15;
  const int h = tid >> 3, col = cg * 32 + (tid & 7) * 4;
  f32x4 o;
  if (prob == 0) o = merge_head<NQA, NQA>(ws + WS_YA + ((b * 32 + h) * NQA) * 512 + col, ws + WS_MLA + (b * 32 + h) * NQA * 2);
  else o = merge_head<NSB, 8>(ws + WS_YB + ((b * 32 + h) * NSB) * 512 + col, ws + WS_MLB + (b * 32 + h) * 16);
#pragma unroll
  for (int i = 0; i < 4; ++i) { float x = o[i]; x += dppf<ROR8>(x); o[i] = xrow_sum(x); }
  if (lane < 8) *(f32x4*)(red + w * 32 + lane * 4) = o;
  __syncthreads();
  if (tid < 16) {
    const float s0 = (red[2 * tid] + red[32 + 2 * tid]) + (red[64 + 2 * tid] + red[96 + 2 * tid]);
    const float s1 = (red[2 * tid + 1] + red[33 + 2 * tid]) + (red[65 + 2 * tid] + red[97 + 2 * tid]);
    *(unsigned*)(out + b * 1024 + prob * 512 + cg * 32 + 2 * tid) = cvtpk(s0, s1);
  }
}

extern "C" void kernel_launch(void* const* d_in, const int* in_sizes, int n_in, void* d_out, int out_size, void* d_ws, size_t ws_size,
                              hipStream_t stream) {
  typedef const unsigned short* cp;
  attn_main<<<256, 512, 0, stream>>>((cp)d_in[0], (cp)d_in[1], (cp)d_in[2], (cp)d_in[3], (cp)d_in[4], (cp)d_in[5], (cp)d_in[6], (cp)d_in[7],
                                     (cp)d_in[8], (cp)d_in[9], (float*)d_ws);
  attn_final<<<64, 256, 0, stream>>>((const float*)d_ws, (unsigned short*)d_out);
}
```

```cpp
#include <hip/hip_runtime.h>
#include <hip/hip_bf16.h>
#include <stdint.h>

typedef unsigned u32x4 __attribute__((ext_vector_type(4)));
typedef float f32x4 __attribute__((ext_vector_type(4)));
typedef float f32x2 __attribute__((ext_vector_type(2)));
typedef __bf16 bf16x2 __attribute__((ext_vector_type(2)));
typedef __amdgpu_buffer_rsrc_t rsrc_t;

#define LPT_B 4
#define AUX_KV 2
#define AUX_W 2
#define NS_B 2
#define DEP_B 1
#define PRE_B 0
#define WO_STEPS 8
#define PATTERN 0
#define ROT_B 0
#define LOCKSTEP 0
#define HMAP (4 * xcd + j / NSB)
#define KVMAP (xcd ^ 1)

constexpr int SK = 16384, WIN = 4096;
constexpr int NSB = 7;
constexpr int NQA = 4;
constexpr float QK_SCALE = 0.08838834764831845f, LOG2E = 1.4426950408889634f;
constexpr float M_INIT = -1e30f;

constexpr int WS_YB = 0;
constexpr int WS_YA = WS_YB + 2 * 32 * NSB * 512;
constexpr int WS_MLB = WS_YA + 2 * 32 * NQA * 512;
constexpr int WS_MLA = WS_MLB + 2 * 32 * 8 * 2;

constexpr int L_QP = 0;
constexpr int L_REC = L_QP + 8192;
constexpr int L_ML = L_REC + 4096;
constexpr int L_OPK = L_ML + 64;
constexpr int L_TOTAL = 24576;

#define TRACE(n)
#define TRACE_DUMP(cnt)
#define STAMP(k)
#define STAMP_T(var)
#define STAMP_W(k, var)
#define INL __attribute__((always_inline))
#define PIN() do { asm volatile("" ::: "memory"); __builtin_amdgcn_sched_barrier(0); } while (0)

__device__ __forceinline__ float dot2(unsigned a, unsigned b, float c) {
  return __builtin_amdgcn_fdot2_f32_bf16(__builtin_bit_cast(bf16x2, a), __builtin_bit_cast(bf16x2, b), c, false);
}
__device__ __forceinline__ unsigned pk_lo(unsigned a, unsigned b) { return __builtin_amdgcn_perm(b, a, 0x05040100u); }
__device__ __forceinline__ unsigned pk_hi(unsigned a, unsigned b) { return __builtin_amdgcn_perm(b, a, 0x07060302u); }
__device__ __forceinline__ unsigned cvtpk(float lo, float hi) {
  bf16x2 r; r[0] = (__bf16)lo; r[1] = (__bf16)hi; return __builtin_bit_cast(unsigned, r);
}
template <int CTRL> __device__ __forceinline__ float dppf(float x) {
  return __builtin_bit_cast(float, __builtin_amdgcn_mov_dpp(__builtin_bit_cast(int, x), CTRL, 0xf, 0xf, true));
}
template <int CTRL> __device__ __forceinline__ unsigned dppu(unsigned x) { return (unsigned)__builtin_amdgcn_mov_dpp((int)x, CTRL, 0xf, 0xf, true); }
constexpr int XOR1 = 0xB1, XOR2 = 0x4E, XOR7 = 0x141, ROR8 = 0x128, ROR4 = 0x124, ROR12 = 0x12C, BCAST = 0x150;
__device__ __forceinline__ float row_sum(float x) {
  x += dppf<ROR8>(x); x += dppf<XOR7>(x); x += dppf<XOR2>(x); x += dppf<XOR1>(x); return x;
}
__device__ __forceinline__ float swadd32(float a, float b) {
  auto s = __builtin_amdgcn_permlane32_swap(__float_as_uint(a), __float_as_uint(b), false, false);
  return __uint_as_float(s[0]) + __uint_as_float(s[1]);
}
__device__ __forceinline__ float swadd16(float a, float b) {
  auto s = __builtin_amdgcn_permlane16_swap(__float_as_uint(a), __float_as_uint(b), false, false);
  return __uint_as_float(s[0]) + __uint_as_float(s[1]);
}
__device__ __forceinline__ float xrow_sum(float x) { x = swadd16(x, x); return swadd32(x, x); }
__device__ __forceinline__ float xrow_max(float x) {
  auto s = __builtin_amdgcn_permlane16_swap(__float_as_uint(x), __float_as_uint(x), false, false);
  x = fmaxf(__uint_as_float(s[0]), __uint_as_float(s[1]));
  auto t = __builtin_amdgcn_permlane32_swap(__float_as_uint(x), __float_as_uint(x), false, false);
  return fmaxf(__uint_as_float(t[0]), __uint_as_float(t[1]));
}
template <int N> struct IC { static constexpr int value = N; };
template <bool B> struct BC { static constexpr bool value = B; };
template <int N, int I = 0, class F> __device__ __forceinline__ void static_for(F&& f) {
  if constexpr (I < N) { f(IC<I>{}); static_for<N, I + 1>(f); }
}
__device__ __forceinline__ void round_scores(float& a, float& b) {
  unsigned u = cvtpk(a, b);
  u = cvtpk(__uint_as_float(u << 16) * QK_SCALE, __uint_as_float(u & 0xffff0000u) * QK_SCALE);
  a = __uint_as_float(u << 16); b = __uint_as_float(u & 0xffff0000u);
}
__device__ __forceinline__ float ex2(float x) { return __builtin_amdgcn_exp2f(x); }
__device__ __forceinline__ rsrc_t mkrs(const void* p, int bytes) { return __builtin_amdgcn_make_buffer_rsrc((void*)p, 0, bytes, 0x00020000); }
template <int AUX> __device__ __forceinline__ u32x4 bld(rsrc_t rs, int vo, int so) { return __builtin_amdgcn_raw_buffer_load_b128(rs, vo, so, AUX); }

__device__ __forceinline__ void gemv16(const u32x4 (&W)[16], const u32x4 (&x0)[2], const u32x4 (&x1)[2], float (&a)[16]) {
#pragma unroll
  for (int k = 0; k < 16; ++k) a[k] = 0.f;
#pragma unroll
  for (int k = 0; k < 8; ++k) {
    const unsigned p0 = x0[k >> 2][k & 3], p1 = x1[k >> 2][k & 3];
#pragma unroll
    for (int j = 0; j < 4; ++j) {
      const unsigned lo = pk_lo(W[2 * k][j], W[2 * k + 1][j]), hi = pk_hi(W[2 * k][j], W[2 * k + 1][j]);
      a[2 * j] = dot2(lo, p0, a[2 * j]); a[2 * j + 1] = dot2(hi, p0, a[2 * j + 1]);
      a[8 + 2 * j] = dot2(lo, p1, a[8 + 2 * j]); a[8 + 2 * j + 1] = dot2(hi, p1, a[8 + 2 * j + 1]);
    }
  }
}
__device__ __forceinline__ f32x4 red54(const float (&a)[16]) {
  float t[8];
#pragma unroll
  for (int i = 0; i < 8; ++i) t[i] = swadd32(a[i], a[i + 8]);
  f32x4 u;
#pragma unroll
  for (int i = 0; i < 4; ++i) u[i] = swadd16(t[i], t[i + 4]);
  return u;
}
__device__ __forceinline__ void wq_issue(u32x4 (&W)[16], rsrc_t rs, int vo, int so) {
#pragma unroll
  for (int i = 0; i < 16; ++i) W[i] = bld<AUX_W>(rs, vo, so + i * 8192);
}
__device__ __forceinline__ void wo_issue(u32x4 (&W)[16], rsrc_t rs, int vo, int so) {
#pragma unroll
  for (int i = 0; i < 16; ++i) W[i] = bld<AUX_W>(rs, vo, so + i * 1024);
}
__device__ __forceinline__ u32x4 q_gather(const float* qp, int b, int r, int c) {
  const float* p = qp + (2 * r) * 256 + b * 128 + 8 * c;
  const f32x4 a0 = *(const f32x4*)p, a1 = *(const f32x4*)(p + 4), b0 = *(const f32x4*)(p + 256), b1 = *(const f32x4*)(p + 260);
  float v[8];
#pragma unroll
  for (int i = 0; i < 4; ++i) { v[i] = xrow_sum(a0[i] + b0[i]); v[4 + i] = xrow_sum(a1[i] + b1[i]); }
  return u32x4{cvtpk(v[0], v[1]), cvtpk(v[2], v[3]), cvtpk(v[4], v[5]), cvtpk(v[6], v[7])};
}
__device__ __forceinline__ void oproj_store(const u32x4 (&W)[16], const unsigned* opk0, const unsigned* opk1, float* y0, float* y1, int w, int lane) {
  const int rr = lane >> 3;
  u32x4 o0[2], o1[2];
  o0[0] = *(const u32x4*)(opk0 + 8 * rr); o0[1] = *(const u32x4*)(opk0 + 8 * rr + 4);
  o1[0] = *(const u32x4*)(opk1 + 8 * rr); o1[1] = *(const u32x4*)(opk1 + 8 * rr + 4);
  float a[16];
  gemv16(W, o0, o1, a);
  f32x4 u = red54(a);
#pragma unroll
  for (int i = 0; i < 4; ++i) u[i] += dppf<ROR8>(u[i]);
  float* y = (lane >> 5) ? y1 : y0;
  if (!(lane & 8)) *(f32x4*)(y + 64 * w + 8 * (lane & 7) + 4 * ((lane >> 4) & 1)) = u;
}

__global__ void __launch_bounds__(512, 2)
attn_main(const unsigned short* Ax, const unsigned short* Ak, const unsigned short* Av, const unsigned short* Awq, const unsigned short* Awo,
          const unsigned short* Bx, const unsigned short* Bk, const unsigned short* Bv, const unsigned short* Bwq, const unsigned short* Bwo, float* ws) {
  __shared__ __attribute__((aligned(16))) float lds[L_TOTAL];
  const int bid = blockIdx.x, tid = threadIdx.x, lane = tid & 63;
  const int w = __builtin_amdgcn_readfirstlane(tid >> 6);
  const int r = lane >> 4, c = lane & 15;
  const int xcd = bid & 7, j = bid >> 3;
  const int b = w >> 2, wb = w & 3;
  const int vo_kv = lane * 16;
  const int vo_wq = r * (16 * 8192) + c * 16;
  const int vo_wo = (lane >> 3) * (16 * 1024) + (lane & 7) * 16;
  unsigned* opk = (unsigned*)(lds + L_OPK);
  STAMP_T(ts0);

  if (j < 4 * NSB) {
    constexpr int LPT = LPT_B, TK = 4 * LPT, TB = TK * 256, G = NSB * 4;
    constexpr int NFULL = SK / (TK * G), REST = (SK - NFULL * TK * G) / 4;
    constexpr int NS = NS_B, DEP = DEP_B, TPI = NS / 2;
    constexpr int NIT = (NFULL + 1 + TPI - 1) / TPI;
    const int h = HMAP, s = j % NSB, g = s * 4 + wb;
    const int r0 = (g * REST) / G, r1 = ((g + 1) * REST) / G;
    const int last_off = (NFULL * TK * G + r0 * 4) * 256, nrec = (NFULL * TK * G + r1 * 4) * 256, nv_last = r1 - r0;
    const rsrc_t rx = mkrs(Bx, 2048), rwq = mkrs(Bwq, 512 * 4096 * 2), rwo = mkrs(Bwo, 512 * 4096 * 2);
    const size_t kvbase = (size_t)(b * 32 + h) * SK * 128;
    const rsrc_t rk = mkrs(Bk + kvbase, nrec), rv = mkrs(Bv + kvbase, nrec);
    u32x4 x0[2], x1[2], W[16], KV[NS][LPT];
    const int rot = ROT_B;
    auto tile_off = [&](int n) INL {
      int nr = n + rot; nr = nr >= NFULL ? nr - NFULL : nr;
      const int full = PATTERN == 0 ? (g + nr * G) * TB : (PATTERN == 1 ? (s * (NFULL * 4) + nr * 4 + wb) * TB : (g * NFULL + nr) * TB);
      return n < NFULL ? full : (n == NFULL ? last_off : SK * 256);
    };
    auto issue = [&](auto slot, int n) INL {
      constexpr int SL = decltype(slot)::value;
      const int so = tile_off(n);
#pragma unroll
      for (int i = 0; i < LPT; ++i) KV[SL][i] = bld<AUX_KV>((SL & 1) ? rv : rk, vo_kv + i * 1024, so);
      PIN();
    };
    static_for<PRE_B>([&](auto i) INL { issue(i, decltype(i)::value / 2); });
    x0[0] = bld<0>(rx, r * 32, w * 128); x0[1] = bld<0>(rx, r * 32 + 16, w * 128);
    x1[0] = bld<0>(rx, r * 32, 1024 + w * 128); x1[1] = bld<0>(rx, r * 32 + 16, 1024 + w * 128);
    wq_issue(W, rwq, vo_wq, w * (64 * 8192) + h * 256);
    PIN();
    static_for<NS, PRE_B>([&](auto i) INL { issue(i, decltype(i)::value / 2); });
    {
      float a[16];
      gemv16(W, x0, x1, a);
      const f32x4 u = red54(a);
      *(f32x4*)(lds + L_QP + w * 256 + (r >> 1) * 128 + 8 * c + 4 * (r & 1)) = u;
    }
    PIN();
    __syncthreads();
    const u32x4 q = q_gather(lds + L_QP, b, r, c);
    STAMP_T(ts1);
    float m = M_INIT, l = 0.f, acc[8];
#pragma unroll
    for (int k = 0; k < 8; ++k) acc[k] = 0.f;
    auto iter = [&](auto first, int n0) INL {
      constexpr bool FIRST = decltype(first)::value;
      static_for<TPI>([&](auto uu) INL {
        constexpr int U = decltype(uu)::value, IK = 2 * U, IV = 2 * U + 1;
        const int n = n0 + U;
        u32x4 (&K)[LPT] = KV[IK];
        u32x4 (&V)[LPT] = KV[IV];
        if constexpr (DEP < NS && !FIRST) {
#pragma unroll
          for (int i = 0; i < LPT; ++i) asm volatile("" : "+v"(K[i]) :: "memory");
          __builtin_amdgcn_sched_barrier(0);
          issue(IC<(IK + DEP) % NS>{}, n0 + (IK + DEP) / 2);
        }
        float sc[LPT];
#pragma unroll
        for (int i = 0; i < LPT; ++i) {
          float d = dot2(K[i][0], q[0], 0.f);
          d = dot2(K[i][1], q[1], d); d = dot2(K[i][2], q[2], d); sc[i] = dot2(K[i][3], q[3], d);
        }
#pragma unroll
        for (int i = 0; i < LPT; ++i) asm volatile("" : "+v"(sc[i]) :: "memory");
        __builtin_amdgcn_sched_barrier(0);
        if constexpr (DEP == NS || (FIRST && IK < DEP)) issue(IC<IK>{}, n + TPI);
        const int nv = n < NFULL ? LPT : (n == NFULL ? nv_last : 0);
        float mx = M_INIT;
#pragma unroll
        for (int i = 0; i < LPT; ++i) sc[i] = row_sum(sc[i]);
#pragma unroll
        for (int i = 0; i < LPT; i += 2) round_scores(sc[i], sc[i + 1]);
#pragma unroll
        for (int i = 0; i < LPT; ++i) { sc[i] = i < nv ? sc[i] : -INFINITY; mx = fmaxf(mx, sc[i]); }
        if (__any(mx > m)) {
          const float mn = fmaxf(m, mx), al = ex2((m - mn) * LOG2E);
          m = mn; l *= al;
#pragma unroll
          for (int k = 0; k < 8; ++k) acc[k] *= al;
        }
        const float mc = m * LOG2E;
        float p[LPT];
#pragma unroll
        for (int i = 0; i < LPT; ++i) { p[i] = ex2(fmaf(sc[i], LOG2E, -mc)); l += p[i]; }
        if constexpr (DEP < NS && !FIRST) {
#pragma unroll
          for (int i = 0; i < LPT; ++i) asm volatile("" : "+v"(V[i]), "+v"(p[i]) :: "memory");
          __builtin_amdgcn_sched_barrier(0);
          issue(IC<(IV + DEP) % NS>{}, n0 + (IV + DEP) / 2);
        }
#pragma unroll
        for (int i = 0; i < LPT; i += 2) {
          const unsigned pp = cvtpk(p[i], p[i + 1]);
#pragma unroll
          for (int jj = 0; jj < 4; ++jj) {
            acc[2 * jj] = dot2(pk_lo(V[i][jj], V[i + 1][jj]), pp, acc[2 * jj]);
            acc[2 * jj + 1] = dot2(pk_hi(V[i][jj], V[i + 1][jj]), pp, acc[2 * jj + 1]);
          }
        }
#pragma unroll
        for (int k = 0; k < 8; ++k) asm volatile("" : "+v"(acc[k]) :: "memory");
        __builtin_amdgcn_sched_barrier(0);
        if constexpr (DEP == NS || (FIRST && IV < DEP)) issue(IC<IV>{}, n + TPI);
        TRACE(n);
        if constexpr (LOCKSTEP) __builtin_amdgcn_s_barrier();
      });
    };
    iter(BC<true>{}, 0);
    static_for<WO_STEPS>([&](auto kk) INL {
      constexpr int KK = decltype(kk)::value, PER = 16 / WO_STEPS;
#pragma unroll
      for (int i = KK * PER; i < (KK + 1) * PER; ++i) W[i] = bld<AUX_W>(rwo, vo_wo, h * (128 * 1024) + w * 128 + i * 1024);
      PIN();
      iter(BC<false>{}, (KK + 1) * TPI);
    });
    for (int it = WO_STEPS + 1; it < NIT; ++it) iter(BC<false>{}, it * TPI);
    STAMP_T(ts2);
    {
      const float mm = xrow_max(m), sc = ex2((m - mm) * LOG2E);
      l = xrow_sum(l * sc);
      float t[4];
#pragma unroll
      for (int i = 0; i < 4; ++i) t[i] = swadd32(acc[i] * sc, acc[i + 4] * sc);
      f32x2 u; u[0] = swadd16(t[0], t[2]); u[1] = swadd16(t[1], t[3]);
      *(f32x2*)(lds + L_REC + w * 512 + 8 * c + 4 * (r >> 1) + 2 * (r & 1)) = u;
      if (lane == 0) *(f32x2*)(lds + L_ML + w * 8) = f32x2{mm, l};
    }
    __syncthreads();
    if (wb == 0) {
      f32x2 ml[4];
#pragma unroll
      for (int i = 0; i < 4; ++i) ml[i] = *(const f32x2*)(lds + L_ML + (w + i) * 8);
      const float M = fmaxf(fmaxf(ml[0][0], ml[1][0]), fmaxf(ml[2][0], ml[3][0]));
      float L = 0.f, o0 = 0.f, o1 = 0.f;
#pragma unroll
      for (int i = 0; i < 4; ++i) {
        const float e = ex2((ml[i][0] - M) * LOG2E);
        const f32x2 v = *(const f32x2*)(lds + L_REC + (w + i) * 512 + 2 * lane);
        L = fmaf(e, ml[i][1], L); o0 = fmaf(e, v[0], o0); o1 = fmaf(e, v[1], o1);
      }
      opk[b * 256 + lane] = cvtpk(o0, o1);
      if (lane == 0) *(f32x2*)(ws + WS_MLB + ((b * 32 + h) * 8 + s) * 2) = f32x2{M, L};
    }
    __syncthreads();
    oproj_store(W, opk, opk + 256, ws + WS_YB + (h * NSB + s) * 512, ws + WS_YB + ((32 + h) * NSB + s) * 512, w, lane);
    STAMP_W(0, ts0); STAMP_W(1, ts1); STAMP_W(2, ts2); STAMP(3);
    TRACE_DUMP(NFULL);
  } else {
    constexpr int TB = 16 * 256;
    const int kvh = KVMAP, qt = j - 4 * NSB;
    const rsrc_t rx = mkrs(Ax, 2048), rwq = mkrs(Awq, 512 * 4096 * 2), rwo = mkrs(Awo, 512 * 4096 * 2);
    const size_t kvbase = ((size_t)(b * 8 + kvh) * SK + (SK - WIN) + qt * (WIN / NQA)) * 128;
    const rsrc_t rk = mkrs(Ak + kvbase, (WIN / NQA) * 256), rv = mkrs(Av + kvbase, (WIN / NQA) * 256);
    u32x4 x0[2], x1[2], W0[16], W1[16], K[4], V[4];
    x0[0] = bld<0>(rx, r * 32, w * 128); x0[1] = bld<0>(rx, r * 32 + 16, w * 128);
    x1[0] = bld<0>(rx, r * 32, 1024 + w * 128); x1[1] = bld<0>(rx, r * 32 + 16, 1024 + w * 128);
    const int sq = w * (64 * 8192) + kvh * 1024;
    float* qdst = lds + L_QP + w * 256 + (r >> 1) * 128 + 8 * c + 4 * (r & 1);
    wq_issue(W0, rwq, vo_wq, sq);
    PIN();
    wq_issue(W1, rwq, vo_wq, sq + 256);
    PIN();
    { float a[16]; gemv16(W0, x0, x1, a); *(f32x4*)(qdst) = red54(a); }
    PIN();
    wq_issue(W0, rwq, vo_wq, sq + 512);
    PIN();
    { float a[16]; gemv16(W1, x0, x1, a); *(f32x4*)(qdst + 2048) = red54(a); }
    PIN();
    wq_issue(W1, rwq, vo_wq, sq + 768);
    PIN();
    int so = wb * TB;
#pragma unroll
    for (int i = 0; i < 4; ++i) K[i] = bld<AUX_KV>(rk, vo_kv + i * 1024, so);
    PIN();
#pragma unroll
    for (int i = 0; i < 4; ++i) V[i] = bld<AUX_KV>(rv, vo_kv + i * 1024, so);
    PIN();
    { float a[16]; gemv16(W0, x0, x1, a); *(f32x4*)(qdst + 4096) = red54(a); }
    PIN();
    { float a[16]; gemv16(W1, x0, x1, a); *(f32x4*)(qdst + 6144) = red54(a); }
    PIN();
    const int swo = kvh * (512 * 1024) + w * 128;
    wo_issue(W0, rwo, vo_wo, swo);
    PIN();
    __syncthreads();
    u32x4 q[4];
#pragma unroll
    for (int hh = 0; hh < 4; ++hh) q[hh] = q_gather(lds + L_QP + hh * 2048, b, r, c);
    STAMP_T(ts1);
    float m = M_INIT, l = 0.f, acc[32];
#pragma unroll
    for (int k = 0; k < 32; ++k) acc[k] = 0.f;
    constexpr int NTA = WIN / NQA / 16 / 4;
    for (int t = 0; t < NTA; ++t) {
      so += 4 * TB;
      float x[16];
#pragma unroll
      for (int i = 0; i < 4; ++i)
#pragma unroll
        for (int hh = 0; hh < 4; ++hh) {
          float d = dot2(K[i][0], q[hh][0], 0.f);
          d = dot2(K[i][1], q[hh][1], d); d = dot2(K[i][2], q[hh][2], d); x[i * 4 + hh] = dot2(K[i][3], q[hh][3], d);
        }
#pragma unroll
      for (int i = 0; i < 16; ++i) asm volatile("" : "+v"(x[i]) :: "memory");
      __builtin_amdgcn_sched_barrier(0);
#pragma unroll
      for (int i = 0; i < 4; ++i) K[i] = bld<AUX_KV>(rk, vo_kv + i * 1024, so);
      PIN();
#define TR_STEP(HALF, CTRL, BIT) _Pragma("unroll") for (int hh = 0; hh < (HALF); ++hh) { \
        const float lo_ = x[hh], hi_ = x[hh + (HALF)]; const float t1_ = lo_ + dppf<CTRL>(lo_), t2_ = hi_ + dppf<CTRL>(hi_); \
        x[hh] = (c & (BIT)) ? t2_ : t1_; }
      TR_STEP(8, ROR8, 8) TR_STEP(4, XOR7, 4) TR_STEP(2, XOR2, 2) TR_STEP(1, XOR1, 1)
#undef TR_STEP
      float sc = x[0], sc_dummy = x[0];
      round_scores(sc, sc_dummy);
      float mx = fmaxf(sc, dppf<ROR4>(sc)); mx = fmaxf(mx, dppf<ROR8>(mx));
      if (__any(mx > m)) {
        const float mn = fmaxf(m, mx), al = ex2((m - mn) * LOG2E);
        m = mn; l *= al;
        const float a0 = dppf<BCAST + 0>(al), a1 = dppf<BCAST + 1>(al), a2 = dppf<BCAST + 2>(al), a3 = dppf<BCAST + 3>(al);
#pragma unroll
        for (int k = 0; k < 8; ++k) { acc[k] *= a0; acc[8 + k] *= a1; acc[16 + k] *= a2; acc[24 + k] *= a3; }
      }
      const float p = ex2(fmaf(sc, LOG2E, -m * LOG2E));
      l += p;
      const unsigned pp = cvtpk(p, dppf<ROR12>(p));
      unsigned pb[8];
      pb[0] = dppu<BCAST + 0>(pp); pb[1] = dppu<BCAST + 1>(pp); pb[2] = dppu<BCAST + 2>(pp); pb[3] = dppu<BCAST + 3>(pp);
      pb[4] = dppu<BCAST + 8>(pp); pb[5] = dppu<BCAST + 9>(pp); pb[6] = dppu<BCAST + 10>(pp); pb[7] = dppu<BCAST + 11>(pp);
#pragma unroll
      for (int i = 0; i < 2; ++i)
#pragma unroll
        for (int jj = 0; jj < 4; ++jj) {
          const unsigned lo = pk_lo(V[2 * i][jj], V[2 * i + 1][jj]), hi = pk_hi(V[2 * i][jj], V[2 * i + 1][jj]);
#pragma unroll
          for (int hh = 0; hh < 4; ++hh) {
            acc[hh * 8 + 2 * jj] = dot2(lo, pb[i * 4 + hh], acc[hh * 8 + 2 * jj]);
            acc[hh * 8 + 2 * jj + 1] = dot2(hi, pb[i * 4 + hh], acc[hh * 8 + 2 * jj + 1]);
          }
        }
#pragma unroll
      for (int k = 0; k < 32; ++k) asm volatile("" : "+v"(acc[k]) :: "memory");
      __builtin_amdgcn_sched_barrier(0);
#pragma unroll
      for (int i = 0; i < 4; ++i) V[i] = bld<AUX_KV>(rv, vo_kv + i * 1024, so);
      PIN();
      TRACE(t);
    }
    STAMP_T(ts2);
    wo_issue(W1, rwo, vo_wo, swo + 128 * 1024);
    PIN();
    {
      const float mm = xrow_max(m), sc = ex2((m - mm) * LOG2E);
      float ls = l * sc; ls += dppf<ROR4>(ls); ls += dppf<ROR8>(ls); ls = xrow_sum(ls);
      float sh[4];
      sh[0] = dppf<BCAST + 0>(sc); sh[1] = dppf<BCAST + 1>(sc); sh[2] = dppf<BCAST + 2>(sc); sh[3] = dppf<BCAST + 3>(sc);
#pragma unroll
      for (int hh = 0; hh < 4; ++hh) {
        float t[4];
#pragma unroll
        for (int i = 0; i < 4; ++i) t[i] = swadd32(acc[hh * 8 + i] * sh[hh], acc[hh * 8 + i + 4] * sh[hh]);
        f32x2 u; u[0] = swadd16(t[0], t[2]); u[1] = swadd16(t[1], t[3]);
        *(f32x2*)(lds + L_REC + w * 512 + hh * 128 + 8 * c + 4 * (r >> 1) + 2 * (r & 1)) = u;
      }
      if (lane < 4) *(f32x2*)(lds + L_ML + w * 8 + lane * 2) = f32x2{mm, ls};
    }
    __syncthreads();
    {
      const int w0 = b * 4, hh = wb;
      f32x2 ml[4];
#pragma unroll
      for (int i = 0; i < 4; ++i) ml[i] = *(const f32x2*)(lds + L_ML + (w0 + i) * 8 + hh * 2);
      const float M = fmaxf(fmaxf(ml[0][0], ml[1][0]), fmaxf(ml[2][0], ml[3][0]));
      float L = 0.f, o0 = 0.f, o1 = 0.f;
#pragma unroll
      for (int i = 0; i < 4; ++i) {
        const float e = ex2((ml[i][0] - M) * LOG2E);
        const f32x2 v = *(const f32x2*)(lds + L_REC + (w0 + i) * 512 + hh * 128 + 2 * lane);
        L = fmaf(e, ml[i][1], L); o0 = fmaf(e, v[0], o0); o1 = fmaf(e, v[1], o1);
      }
      opk[b * 256 + hh * 64 + lane] = cvtpk(o0, o1);
      if (lane == 0) *(f32x2*)(ws + WS_MLA + ((b * 32 + kvh * 4 + hh) * NQA + qt) * 2) = f32x2{M, L};
    }
    __syncthreads();
    float* y0 = ws + WS_YA + ((kvh * 4) * NQA + qt) * 512;
    float* y1 = y0 + 32 * NQA * 512;
    oproj_store(W0, opk, opk + 256, y0, y1, w, lane);
    PIN();
    wo_issue(W0, rwo, vo_wo, swo + 2 * 128 * 1024);
    PIN();
    oproj_store(W1, opk + 64, opk + 256 + 64, y0 + NQA * 512, y1 + NQA * 512, w, lane);
    PIN();
    wo_issue(W1, rwo, vo_wo, swo + 3 * 128 * 1024);
    PIN();
    oproj_store(W0, opk + 128, opk + 256 + 128, y0 + 2 * NQA * 512, y1 + 2 * NQA * 512, w, lane);
    PIN();
    oproj_store(W1, opk + 192, opk + 256 + 192, y0 + 3 * NQA * 512, y1 + 3 * NQA * 512, w, lane);
    STAMP_W(0, ts0); STAMP_W(1, ts1); STAMP_W(2, ts2); STAMP(3);
    TRACE_DUMP(NTA);
  }
}

template <int NS, int MLS>
__device__ __forceinline__ f32x4 merge_head(const float* y, const float* ml) {
  f32x4 v[NS]; f32x2 e[NS];
#pragma unroll
  for (int s = 0; s < NS; ++s) { v[s] = *(const f32x4*)(y + s * 512); e[s] = *(const f32x2*)(ml + s * 2); }
  float M = e[0][0];
#pragma unroll
  for (int s = 1; s < NS; ++s) M = fmaxf(M, e[s][0]);
  float L = 0.f; f32x4 o = {0.f, 0.f, 0.f, 0.f};
#pragma unroll
  for (int s = 0; s < NS; ++s) { const float wt = ex2((e[s][0] - M) * LOG2E); L = fmaf(wt, e[s][1], L); o += wt * v[s]; }
  return o * (1.f / L);
}
__global__ void __launch_bounds__(256) attn_final(float* ws, unsigned short* out) {
  STAMP_T(ts0);
  __shared__ __attribute__((aligned(16))) float red[4 * 32];
  const int bid = blockIdx.x, tid = threadIdx.x, lane = tid & 63, w = tid >> 6;
  const int prob = bid >> 5, b = (bid >> 4) & 1, cg = bid & 15;
  const int h = tid >> 3, col = cg * 32 + (tid & 7) * 4;
  f32x4 o;
  if (prob == 0) o = merge_head<NQA, NQA>(ws + WS_YA + ((b * 32 + h) * NQA) * 512 + col, ws + WS_MLA + (b * 32 + h) * NQA * 2);
  else o = merge_head<NSB, 8>(ws + WS_YB + ((b * 32 + h) * NSB) * 512 + col, ws + WS_MLB + (b * 32 + h) * 16);
#pragma unroll
  for (int i = 0; i < 4; ++i) { float x = o[i]; x += dppf<ROR8>(x); o[i] = xrow_sum(x); }
  if (lane < 8) *(f32x4*)(red + w * 32 + lane * 4) = o;
  __syncthreads();
  if (tid < 16) {
    const float s0 = (red[2 * tid] + red[32 + 2 * tid]) + (red[64 + 2 * tid] + red[96 + 2 * tid]);
    const float s1 = (red[2 * tid + 1] + red[33 + 2 * tid]) + (red[65 + 2 * tid] + red[97 + 2 * tid]);
    *(unsigned*)(out + b * 1024 + prob * 512 + cg * 32 + 2 * tid) = cvtpk(s0, s1);
  }
  STAMP_W(4, ts0); STAMP(5);
}

extern "C" void kernel_launch(void* const* d_in, const int* in_sizes, int n_in, void* d_out, int out_size, void* d_ws, size_t ws_size,
                              hipStream_t stream) {
  typedef const unsigned short* cp;
  attn_main<<<256, 512, 0, stream>>>((cp)d_in[0], (cp)d_in[1], (cp)d_in[2], (cp)d_in[3], (cp)d_in[4], (cp)d_in[5], (cp)d_in[6], (cp)d_in[7],
                                     (cp)d_in[8], (cp)d_in[9], (float*)d_ws);
  attn_final<<<64, 256, 0, stream>>>((float*)d_ws, (unsigned short*)d_out);
}
```

```cpp
#define STAMPS
#include <cstdio>
#include <vector>
#include <algorithm>
#include <hip/hip_runtime.h>
#include <hip/hip_bf16.h>
#include <stdint.h>

typedef unsigned u32x4 __attribute__((ext_vector_type(4)));
typedef float f32x4 __attribute__((ext_vector_type(4)));
typedef float f32x2 __attribute__((ext_vector_type(2)));
typedef __bf16 bf16x2 __attribute__((ext_vector_type(2)));
typedef __amdgpu_buffer_rsrc_t rsrc_t;

#define LPT_B 4
#define AUX_KV 2
#define AUX_W 2
#define NS_B 2
#define DEP_B 1
#define PRE_B 0
#define WO_STEPS 8
#define PATTERN 0
#define ROT_B 0
#define LOCKSTEP 0
#define HMAP (4 * xcd + j / NSB)
#define KVMAP (xcd ^ 1)

constexpr int SK = 16384, WIN = 4096;
constexpr int NSB = 7;
constexpr int NQA = 4;
constexpr float QK_SCALE = 0.08838834764831845f, LOG2E = 1.4426950408889634f;
constexpr float M_INIT = -1e30f;

constexpr int WS_YB = 0;
constexpr int WS_YA = WS_YB + 2 * 32 * NSB * 512;
constexpr int WS_MLB = WS_YA + 2 * 32 * NQA * 512;
constexpr int WS_MLA = WS_MLB + 2 * 32 * 8 * 2;

constexpr int L_QP = 0;
constexpr int L_REC = L_QP + 8192;
constexpr int L_ML = L_REC + 4096;
constexpr int L_OPK = L_ML + 64;
constexpr int L_TOTAL = 24576;

#define STAMP_OFF (64u << 20)
#define STAMP(k) do { if (threadIdx.x == 0) ((unsigned long long*)((char*)ws + STAMP_OFF))[blockIdx.x * 8 + (k)] = __builtin_amdgcn_s_memrealtime(); } while (0)
#define STAMP_T(var) const unsigned long long var = __builtin_amdgcn_s_memrealtime()
#define STAMP_W(k, var) do { if (threadIdx.x == 0) ((unsigned long long*)((char*)ws + STAMP_OFF))[blockIdx.x * 8 + (k)] = var; } while (0)
#define TRACE(n) do { unsigned tt_ = (unsigned)__builtin_amdgcn_s_memrealtime(); if (lane == 0) ((unsigned*)lds)[16384 + w * 128 + (n)] = tt_; } while (0)
#define TRACE_DUMP(cnt) do { for (int i_ = lane; i_ < 128; i_ += 64) ((unsigned*)((char*)ws + STAMP_OFF + (1u << 20)))[(blockIdx.x * 8 + w) * 128 + i_] = i_ < (cnt) ? ((unsigned*)lds)[16384 + w * 128 + i_] : 0u; } while (0)
#define INL __attribute__((always_inline))
#define PIN() do { asm volatile("" ::: "memory"); __builtin_amdgcn_sched_barrier(0); } while (0)

__device__ __forceinline__ float dot2(unsigned a, unsigned b, float c) {
  return __builtin_amdgcn_fdot2_f32_bf16(__builtin_bit_cast(bf16x2, a), __builtin_bit_cast(bf16x2, b), c, false);
}
__device__ __forceinline__ unsigned pk_lo(unsigned a, unsigned b) { return __builtin_amdgcn_perm(b, a, 0x05040100u); }
__device__ __forceinline__ unsigned pk_hi(unsigned a, unsigned b) { return __builtin_amdgcn_perm(b, a, 0x07060302u); }
__device__ __forceinline__ unsigned cvtpk(float lo, float hi) {
  bf16x2 r; r[0] = (__bf16)lo; r[1] = (__bf16)hi; return __builtin_bit_cast(unsigned, r);
}
template <int CTRL> __device__ __forceinline__ float dppf(float x) {
  return __builtin_bit_cast(float, __builtin_amdgcn_mov_dpp(__builtin_bit_cast(int, x), CTRL, 0xf, 0xf, true));
}
template <int CTRL> __device__ __forceinline__ unsigned dppu(unsigned x) { return (unsigned)__builtin_amdgcn_mov_dpp((int)x, CTRL, 0xf, 0xf, true); }
constexpr int XOR1 = 0xB1, XOR2 = 0x4E, XOR7 = 0x141, ROR8 = 0x128, ROR4 = 0x124, ROR12 = 0x12C, BCAST = 0x150;
__device__ __forceinline__ float row_sum(float x) {
  x += dppf<ROR8>(x); x += dppf<XOR7>(x); x += dppf<XOR2>(x); x += dppf<XOR1>(x); return x;
}
__device__ __forceinline__ float swadd32(float a, float b) {
  auto s = __builtin_amdgcn_permlane32_swap(__float_as_uint(a), __float_as_uint(b), false, false);
  return __uint_as_float(s[0]) + __uint_as_float(s[1]);
}
__device__ __forceinline__ float swadd16(float a, float b) {
  auto s = __builtin_amdgcn_permlane16_swap(__float_as_uint(a), __float_as_uint(b), false, false);
  return __uint_as_float(s[0]) + __uint_as_float(s[1]);
}
__device__ __forceinline__ float xrow_sum(float x) { x = swadd16(x, x); return swadd32(x, x); }
__device__ __forceinline__ float xrow_max(float x) {
  auto s = __builtin_amdgcn_permlane16_swap(__float_as_uint(x), __float_as_uint(x), false, false);
  x = fmaxf(__uint_as_float(s[0]), __uint_as_float(s[1]));
  auto t = __builtin_amdgcn_permlane32_swap(__float_as_uint(x), __float_as_uint(x), false, false);
  return fmaxf(__uint_as_float(t[0]), __uint_as_float(t[1]));
}
template <int N> struct IC { static constexpr int value = N; };
template <bool B> struct BC { static constexpr bool value = B; };
template <int N, int I = 0, class F> __device__ __forceinline__ void static_for(F&& f) {
  if constexpr (I < N) { f(IC<I>{}); static_for<N, I + 1>(f); }
}
__device__ __forceinline__ void round_scores(float& a, float& b) {
  unsigned u = cvtpk(a, b);
  u = cvtpk(__uint_as_float(u << 16) * QK_SCALE, __uint_as_float(u & 0xffff0000u) * QK_SCALE);
  a = __uint_as_float(u << 16); b = __uint_as_float(u & 0xffff0000u);
}
__device__ __forceinline__ float ex2(float x) { return __builtin_amdgcn_exp2f(x); }
__device__ __forceinline__ rsrc_t mkrs(const void* p, int bytes) { return __builtin_amdgcn_make_buffer_rsrc((void*)p, 0, bytes, 0x00020000); }
template <int AUX> __device__ __forceinline__ u32x4 bld(rsrc_t rs, int vo, int so) { return __builtin_amdgcn_raw_buffer_load_b128(rs, vo, so, AUX); }

__device__ __forceinline__ void gemv16(const u32x4 (&W)[16], const u32x4 (&x0)[2], const u32x4 (&x1)[2], float (&a)[16]) {
#pragma unroll
  for (int k = 0; k < 16; ++k) a[k] = 0.f;
#pragma unroll
  for (int k = 0; k < 8; ++k) {
    const unsigned p0 = x0[k >> 2][k & 3], p1 = x1[k >> 2][k & 3];
#pragma unroll
    for (int j = 0; j < 4; ++j) {
      const unsigned lo = pk_lo(W[2 * k][j], W[2 * k + 1][j]), hi = pk_hi(W[2 * k][j], W[2 * k + 1][j]);
      a[2 * j] = dot2(lo, p0, a[2 * j]); a[2 * j + 1] = dot2(hi, p0, a[2 * j + 1]);
      a[8 + 2 * j] = dot2(lo, p1, a[8 + 2 * j]); a[8 + 2 * j + 1] = dot2(hi, p1, a[8 + 2 * j + 1]);
    }
  }
}
__device__ __forceinline__ f32x4 red54(const float (&a)[16]) {
  float t[8];
#pragma unroll
  for (int i = 0; i < 8; ++i) t[i] = swadd32(a[i], a[i + 8]);
  f32x4 u;
#pragma unroll
  for (int i = 0; i < 4; ++i) u[i] = swadd16(t[i], t[i + 4]);
  return u;
}
__device__ __forceinline__ void wq_issue(u32x4 (&W)[16], rsrc_t rs, int vo, int so) {
#pragma unroll
  for (int i = 0; i < 16; ++i) W[i] = bld<AUX_W>(rs, vo, so + i * 8192);
}
__device__ __forceinline__ void wo_issue(u32x4 (&W)[16], rsrc_t rs, int vo, int so) {
#pragma unroll
  for (int i = 0; i < 16; ++i) W[i] = bld<AUX_W>(rs, vo, so + i * 1024);
}
__device__ __forceinline__ u32x4 q_gather(const float* qp, int b, int r, int c) {
  const float* p = qp + (2 * r) * 256 + b * 128 + 8 * c;
  const f32x4 a0 = *(const f32x4*)p, a1 = *(const f32x4*)(p + 4), b0 = *(const f32x4*)(p + 256), b1 = *(const f32x4*)(p + 260);
  float v[8];
#pragma unroll
  for (int i = 0; i < 4; ++i) { v[i] = xrow_sum(a0[i] + b0[i]); v[4 + i] = xrow_sum(a1[i] + b1[i]); }
  return u32x4{cvtpk(v[0], v[1]), cvtpk(v[2], v[3]), cvtpk(v[4], v[5]), cvtpk(v[6], v[7])};
}
__device__ __forceinline__ void oproj_store(const u32x4 (&W)[16], const unsigned* opk0, const unsigned* opk1, float* y0, float* y1, int w, int lane) {
  const int rr = lane >> 3;
  u32x4 o0[2], o1[2];
  o0[0] = *(const u32x4*)(opk0 + 8 * rr); o0[1] = *(const u32x4*)(opk0 + 8 * rr + 4);
  o1[0] = *(const u32x4*)(opk1 + 8 * rr); o1[1] = *(const u32x4*)(opk1 + 8 * rr + 4);
  float a[16];
  gemv16(W, o0, o1, a);
  f32x4 u = red54(a);
#pragma unroll
  for (int i = 0; i < 4; ++i) u[i] += dppf<ROR8>(u[i]);
  float* y = (lane >> 5) ? y1 : y0;
  if (!(lane & 8)) *(f32x4*)(y + 64 * w + 8 * (lane & 7) + 4 * ((lane >> 4) & 1)) = u;
}

__global__ void __launch_bounds__(512, 2)
attn_main(const unsigned short* Ax, const unsigned short* Ak, const unsigned short* Av, const unsigned short* Awq, const unsigned short* Awo,
          const unsigned short* Bx, const unsigned short* Bk, const unsigned short* Bv, const unsigned short* Bwq, const unsigned short* Bwo, float* ws) {
  __shared__ __attribute__((aligned(16))) float lds[L_TOTAL];
  const int bid = blockIdx.x, tid = threadIdx.x, lane = tid & 63;
  const int w = __builtin_amdgcn_readfirstlane(tid >> 6);
  const int r = lane >> 4, c = lane & 15;
  const int xcd = bid & 7, j = bid >> 3;
  const int b = w >> 2, wb = w & 3;
  const int vo_kv = lane * 16;
  const int vo_wq = r * (16 * 8192) + c * 16;
  const int vo_wo = (lane >> 3) * (16 * 1024) + (lane & 7) * 16;
  unsigned* opk = (unsigned*)(lds + L_OPK);
  STAMP_T(ts0);

  if (j < 4 * NSB) {
    constexpr int LPT = LPT_B, TK = 4 * LPT, TB = TK * 256, G = NSB * 4;
    constexpr int NFULL = SK / (TK * G), REST = (SK - NFULL * TK * G) / 4;
    constexpr int NS = NS_B, DEP = DEP_B, TPI = NS / 2;
    constexpr int NIT = (NFULL + 1 + TPI - 1) / TPI;
    const int h = HMAP, s = j % NSB, g = s * 4 + wb;
    const int r0 = (g * REST) / G, r1 = ((g + 1) * REST) / G;
    const int last_off = (NFULL * TK * G + r0 * 4) * 256, nrec = (NFULL * TK * G + r1 * 4) * 256, nv_last = r1 - r0;
    const rsrc_t rx = mkrs(Bx, 2048), rwq = mkrs(Bwq, 512 * 4096 * 2), rwo = mkrs(Bwo, 512 * 4096 * 2);
    const size_t kvbase = (size_t)(b * 32 + h) * SK * 128;
    const rsrc_t rk = mkrs(Bk + kvbase, nrec), rv = mkrs(Bv + kvbase, nrec);
    u32x4 x0[2], x1[2], W[16], KV[NS][LPT];
    const int rot = ROT_B;
    auto tile_off = [&](int n) INL {
      int nr = n + rot; nr = nr >= NFULL ? nr - NFULL : nr;
      const int full = PATTERN == 0 ? (g + nr * G) * TB : (PATTERN == 1 ? (s * (NFULL * 4) + nr * 4 + wb) * TB : (g * NFULL + nr) * TB);
      return n < NFULL ? full : (n == NFULL ? last_off : SK * 256);
    };
    auto issue = [&](auto slot, int n) INL {
      constexpr int SL = decltype(slot)::value;
      const int so = tile_off(n);
#pragma unroll
      for (int i = 0; i < LPT; ++i) KV[SL][i] = bld<AUX_KV>((SL & 1) ? rv : rk, vo_kv + i * 1024, so);
      PIN();
    };
    static_for<PRE_B>([&](auto i) INL { issue(i, decltype(i)::value / 2); });
    x0[0] = bld<0>(rx, r * 32, w * 128); x0[1] = bld<0>(rx, r * 32 + 16, w * 128);
    x1[0] = bld<0>(rx, r * 32, 1024 + w * 128); x1[1] = bld<0>(rx, r * 32 + 16, 1024 + w * 128);
    wq_issue(W, rwq, vo_wq, w * (64 * 8192) + h * 256);
    PIN();
    static_for<NS, PRE_B>([&](auto i) INL { issue(i, decltype(i)::value / 2); });
    {
      float a[16];
      gemv16(W, x0, x1, a);
      const f32x4 u = red54(a);
      *(f32x4*)(lds + L_QP + w * 256 + (r >> 1) * 128 + 8 * c + 4 * (r & 1)) = u;
    }
    PIN();
    __syncthreads();
    const u32x4 q = q_gather(lds + L_QP, b, r, c);
    STAMP_T(ts1);
    float m = M_INIT, l = 0.f, acc[8];
#pragma unroll
    for (int k = 0; k < 8; ++k) acc[k] = 0.f;
    auto iter = [&](auto first, int n0) INL {
      constexpr bool FIRST = decltype(first)::value;
      static_for<TPI>([&](auto uu) INL {
        constexpr int U = decltype(uu)::value, IK = 2 * U, IV = 2 * U + 1;
        const int n = n0 + U;
        u32x4 (&K)[LPT] = KV[IK];
        u32x4 (&V)[LPT] = KV[IV];
        if constexpr (DEP < NS && !FIRST) {
#pragma unroll
          for (int i = 0; i < LPT; ++i) asm volatile("" : "+v"(K[i]) :: "memory");
          __builtin_amdgcn_sched_barrier(0);
          issue(IC<(IK + DEP) % NS>{}, n0 + (IK + DEP) / 2);
        }
        float sc[LPT];
#pragma unroll
        for (int i = 0; i < LPT; ++i) {
          float d = dot2(K[i][0], q[0], 0.f);
          d = dot2(K[i][1], q[1], d); d = dot2(K[i][2], q[2], d); sc[i] = dot2(K[i][3], q[3], d);
        }
#pragma unroll
        for (int i = 0; i < LPT; ++i) asm volatile("" : "+v"(sc[i]) :: "memory");
        __builtin_amdgcn_sched_barrier(0);
        if constexpr (DEP == NS || (FIRST && IK < DEP)) issue(IC<IK>{}, n + TPI);
        const int nv = n < NFULL ? LPT : (n == NFULL ? nv_last : 0);
        float mx = M_INIT;
#pragma unroll
        for (int i = 0; i < LPT; ++i) sc[i] = row_sum(sc[i]);
#pragma unroll
        for (int i = 0; i < LPT; i += 2) round_scores(sc[i], sc[i + 1]);
#pragma unroll
        for (int i = 0; i < LPT; ++i) { sc[i] = i < nv ? sc[i] : -INFINITY; mx = fmaxf(mx, sc[i]); }
        if (__any(mx > m)) {
          const float mn = fmaxf(m, mx), al = ex2((m - mn) * LOG2E);
          m = mn; l *= al;
#pragma unroll
          for (int k = 0; k < 8; ++k) acc[k] *= al;
        }
        const float mc = m * LOG2E;
        float p[LPT];
#pragma unroll
        for (int i = 0; i < LPT; ++i) { p[i] = ex2(fmaf(sc[i], LOG2E, -mc)); l += p[i]; }
        if constexpr (DEP < NS && !FIRST) {
#pragma unroll
          for (int i = 0; i < LPT; ++i) asm volatile("" : "+v"(V[i]), "+v"(p[i]) :: "memory");
          __builtin_amdgcn_sched_barrier(0);
          issue(IC<(IV + DEP) % NS>{}, n0 + (IV + DEP) / 2);
        }
#pragma unroll
        for (int i = 0; i < LPT; i += 2) {
          const unsigned pp = cvtpk(p[i], p[i + 1]);
#pragma unroll
          for (int jj = 0; jj < 4; ++jj) {
            acc[2 * jj] = dot2(pk_lo(V[i][jj], V[i + 1][jj]), pp, acc[2 * jj]);
            acc[2 * jj + 1] = dot2(pk_hi(V[i][jj], V[i + 1][jj]), pp, acc[2 * jj + 1]);
          }
        }
#pragma unroll
        for (int k = 0; k < 8; ++k) asm volatile("" : "+v"(acc[k]) :: "memory");
        __builtin_amdgcn_sched_barrier(0);
        if constexpr (DEP == NS || (FIRST && IV < DEP)) issue(IC<IV>{}, n + TPI);
        TRACE(n);
        if constexpr (LOCKSTEP) __builtin_amdgcn_s_barrier();
      });
    };
    iter(BC<true>{}, 0);
    static_for<WO_STEPS>([&](auto kk) INL {
      constexpr int KK = decltype(kk)::value, PER = 16 / WO_STEPS;
#pragma unroll
      for (int i = KK * PER; i < (KK + 1) * PER; ++i) W[i] = bld<AUX_W>(rwo, vo_wo, h * (128 * 1024) + w * 128 + i * 1024);
      PIN();
      iter(BC<false>{}, (KK + 1) * TPI);
    });
    for (int it = WO_STEPS + 1; it < NIT; ++it) iter(BC<false>{}, it * TPI);
    STAMP_T(ts2);
    if (lane == 0) ((unsigned long long*)((char*)ws + STAMP_OFF))[4096 + blockIdx.x * 8 + w] = ts2;
    {
      const float mm = xrow_max(m), sc = ex2((m - mm) * LOG2E);
      l = xrow_sum(l * sc);
      float t[4];
#pragma unroll
      for (int i = 0; i < 4; ++i) t[i] = swadd32(acc[i] * sc, acc[i + 4] * sc);
      f32x2 u; u[0] = swadd16(t[0], t[2]); u[1] = swadd16(t[1], t[3]);
      *(f32x2*)(lds + L_REC + w * 512 + 8 * c + 4 * (r >> 1) + 2 * (r & 1)) = u;
      if (lane == 0) *(f32x2*)(lds + L_ML + w * 8) = f32x2{mm, l};
    }
    __syncthreads();
    if (wb == 0) {
      f32x2 ml[4];
#pragma unroll
      for (int i = 0; i < 4; ++i) ml[i] = *(const f32x2*)(lds + L_ML + (w + i) * 8);
      const float M = fmaxf(fmaxf(ml[0][0], ml[1][0]), fmaxf(ml[2][0], ml[3][0]));
      float L = 0.f, o0 = 0.f, o1 = 0.f;
#pragma unroll
      for (int i = 0; i < 4; ++i) {
        const float e = ex2((ml[i][0] - M) * LOG2E);
        const f32x2 v = *(const f32x2*)(lds + L_REC + (w + i) * 512 + 2 * lane);
        L = fmaf(e, ml[i][1], L); o0 = fmaf(e, v[0], o0); o1 = fmaf(e, v[1], o1);
      }
      opk[b * 256 + lane] = cvtpk(o0, o1);
      if (lane == 0) *(f32x2*)(ws + WS_MLB + ((b * 32 + h) * 8 + s) * 2) = f32x2{M, L};
    }
    __syncthreads();
    oproj_store(W, opk, opk + 256, ws + WS_YB + (h * NSB + s) * 512, ws + WS_YB + ((32 + h) * NSB + s) * 512, w, lane);
    STAMP_W(0, ts0); STAMP_W(1, ts1); STAMP_W(2, ts2); STAMP(3);
    TRACE_DUMP(NFULL);
  } else {
    constexpr int TB = 16 * 256;
    const int kvh = KVMAP, qt = j - 4 * NSB;
    const rsrc_t rx = mkrs(Ax, 2048), rwq = mkrs(Awq, 512 * 4096 * 2), rwo = mkrs(Awo, 512 * 4096 * 2);
    const size_t kvbase = ((size_t)(b * 8 + kvh) * SK + (SK - WIN) + qt * (WIN / NQA)) * 128;
    const rsrc_t rk = mkrs(Ak + kvbase, (WIN / NQA) * 256), rv = mkrs(Av + kvbase, (WIN / NQA) * 256);
    u32x4 x0[2], x1[2], W0[16], W1[16], K[4], V[4];
    x0[0] = bld<0>(rx, r * 32, w * 128); x0[1] = bld<0>(rx, r * 32 + 16, w * 128);
    x1[0] = bld<0>(rx, r * 32, 1024 + w * 128); x1[1] = bld<0>(rx, r * 32 + 16, 1024 + w * 128);
    const int sq = w * (64 * 8192) + kvh * 1024;
    float* qdst = lds + L_QP + w * 256 + (r >> 1) * 128 + 8 * c + 4 * (r & 1);
    wq_issue(W0, rwq, vo_wq, sq);
    PIN();
    wq_issue(W1, rwq, vo_wq, sq + 256);
    PIN();
    { float a[16]; gemv16(W0, x0, x1, a); *(f32x4*)(qdst) = red54(a); }
    PIN();
    wq_issue(W0, rwq, vo_wq, sq + 512);
    PIN();
    { float a[16]; gemv16(W1, x0, x1, a); *(f32x4*)(qdst + 2048) = red54(a); }
    PIN();
    wq_issue(W1, rwq, vo_wq, sq + 768);
    PIN();
    int so = wb * TB;
#pragma unroll
    for (int i = 0; i < 4; ++i) K[i] = bld<AUX_KV>(rk, vo_kv + i * 1024, so);
    PIN();
#pragma unroll
    for (int i = 0; i < 4; ++i) V[i] = bld<AUX_KV>(rv, vo_kv + i * 1024, so);
    PIN();
    { float a[16]; gemv16(W0, x0, x1, a); *(f32x4*)(qdst + 4096) = red54(a); }
    PIN();
    { float a[16]; gemv16(W1, x0, x1, a); *(f32x4*)(qdst + 6144) = red54(a); }
    PIN();
    const int swo = kvh * (512 * 1024) + w * 128;
    wo_issue(W0, rwo, vo_wo, swo);
    PIN();
    __syncthreads();
    u32x4 q[4];
#pragma unroll
    for (int hh = 0; hh < 4; ++hh) q[hh] = q_gather(lds + L_QP + hh * 2048, b, r, c);
    STAMP_T(ts1);
    float m = M_INIT, l = 0.f, acc[32];
#pragma unroll
    for (int k = 0; k < 32; ++k) acc[k] = 0.f;
    constexpr int NTA = WIN / NQA / 16 / 4;
    for (int t = 0; t < NTA; ++t) {
      so += 4 * TB;
      float x[16];
#pragma unroll
      for (int i = 0; i < 4; ++i)
#pragma unroll
        for (int hh = 0; hh < 4; ++hh) {
          float d = dot2(K[i][0], q[hh][0], 0.f);
          d = dot2(K[i][1], q[hh][1], d); d = dot2(K[i][2], q[hh][2], d); x[i * 4 + hh] = dot2(K[i][3], q[hh][3], d);
        }
#pragma unroll
      for (int i = 0; i < 16; ++i) asm volatile("" : "+v"(x[i]) :: "memory");
      __builtin_amdgcn_sched_barrier(0);
#pragma unroll
      for (int i = 0; i < 4; ++i) K[i] = bld<AUX_KV>(rk, vo_kv + i * 1024, so);
      PIN();
#define TR_STEP(HALF, CTRL, BIT) _Pragma("unroll") for (int hh = 0; hh < (HALF); ++hh) { \
        const float lo_ = x[hh], hi_ = x[hh + (HALF)]; const float t1_ = lo_ + dppf<CTRL>(lo_), t2_ = hi_ + dppf<CTRL>(hi_); \
        x[hh] = (c & (BIT)) ? t2_ : t1_; }
      TR_STEP(8, ROR8, 8) TR_STEP(4, XOR7, 4) TR_STEP(2, XOR2, 2) TR_STEP(1, XOR1, 1)
#undef TR_STEP
      float sc = x[0], sc_dummy = x[0];
      round_scores(sc, sc_dummy);
      float mx = fmaxf(sc, dppf<ROR4>(sc)); mx = fmaxf(mx, dppf<ROR8>(mx));
      if (__any(mx > m)) {
        const float mn = fmaxf(m, mx), al = ex2((m - mn) * LOG2E);
        m = mn; l *= al;
        const float a0 = dppf<BCAST + 0>(al), a1 = dppf<BCAST + 1>(al), a2 = dppf<BCAST + 2>(al), a3 = dppf<BCAST + 3>(al);
#pragma unroll
        for (int k = 0; k < 8; ++k) { acc[k] *= a0; acc[8 + k] *= a1; acc[16 + k] *= a2; acc[24 + k] *= a3; }
      }
      const float p = ex2(fmaf(sc, LOG2E, -m * LOG2E));
      l += p;
      const unsigned pp = cvtpk(p, dppf<ROR12>(p));
      unsigned pb[8];
      pb[0] = dppu<BCAST + 0>(pp); pb[1] = dppu<BCAST + 1>(pp); pb[2] = dppu<BCAST + 2>(pp); pb[3] = dppu<BCAST + 3>(pp);
      pb[4] = dppu<BCAST + 8>(pp); pb[5] = dppu<BCAST + 9>(pp); pb[6] = dppu<BCAST + 10>(pp); pb[7] = dppu<BCAST + 11>(pp);
#pragma unroll
      for (int i = 0; i < 2; ++i)
#pragma unroll
        for (int jj = 0; jj < 4; ++jj) {
          const unsigned lo = pk_lo(V[2 * i][jj], V[2 * i + 1][jj]), hi = pk_hi(V[2 * i][jj], V[2 * i + 1][jj]);
#pragma unroll
          for (int hh = 0; hh < 4; ++hh) {
            acc[hh * 8 + 2 * jj] = dot2(lo, pb[i * 4 + hh], acc[hh * 8 + 2 * jj]);
            acc[hh * 8 + 2 * jj + 1] = dot2(hi, pb[i * 4 + hh], acc[hh * 8 + 2 * jj + 1]);
          }
        }
#pragma unroll
      for (int k = 0; k < 32; ++k) asm volatile("" : "+v"(acc[k]) :: "memory");
      __builtin_amdgcn_sched_barrier(0);
#pragma unroll
      for (int i = 0; i < 4; ++i) V[i] = bld<AUX_KV>(rv, vo_kv + i * 1024, so);
      PIN();
      TRACE(t);
    }
    STAMP_T(ts2);
    wo_issue(W1, rwo, vo_wo, swo + 128 * 1024);
    PIN();
    {
      const float mm = xrow_max(m), sc = ex2((m - mm) * LOG2E);
      float ls = l * sc; ls += dppf<ROR4>(ls); ls += dppf<ROR8>(ls); ls = xrow_sum(ls);
      float sh[4];
      sh[0] = dppf<BCAST + 0>(sc); sh[1] = dppf<BCAST + 1>(sc); sh[2] = dppf<BCAST + 2>(sc); sh[3] = dppf<BCAST + 3>(sc);
#pragma unroll
      for (int hh = 0; hh < 4; ++hh) {
        float t[4];
#pragma unroll
        for (int i = 0; i < 4; ++i) t[i] = swadd32(acc[hh * 8 + i] * sh[hh], acc[hh * 8 + i + 4] * sh[hh]);
        f32x2 u; u[0] = swadd16(t[0], t[2]); u[1] = swadd16(t[1], t[3]);
        *(f32x2*)(lds + L_REC + w * 512 + hh * 128 + 8 * c + 4 * (r >> 1) + 2 * (r & 1)) = u;
      }
      if (lane < 4) *(f32x2*)(lds + L_ML + w * 8 + lane * 2) = f32x2{mm, ls};
    }
    __syncthreads();
    {
      const int w0 = b * 4, hh = wb;
      f32x2 ml[4];
#pragma unroll
      for (int i = 0; i < 4; ++i) ml[i] = *(const f32x2*)(lds + L_ML + (w0 + i) * 8 + hh * 2);
      const float M = fmaxf(fmaxf(ml[0][0], ml[1][0]), fmaxf(ml[2][0], ml[3][0]));
      float L = 0.f, o0 = 0.f, o1 = 0.f;
#pragma unroll
      for (int i = 0; i < 4; ++i) {
        const float e = ex2((ml[i][0] - M) * LOG2E);
        const f32x2 v = *(const f32x2*)(lds + L_REC + (w0 + i) * 512 + hh * 128 + 2 * lane);
        L = fmaf(e, ml[i][1], L); o0 = fmaf(e, v[0], o0); o1 = fmaf(e, v[1], o1);
      }
      opk[b * 256 + hh * 64 + lane] = cvtpk(o0, o1);
      if (lane == 0) *(f32x2*)(ws + WS_MLA + ((b * 32 + kvh * 4 + hh) * NQA + qt) * 2) = f32x2{M, L};
    }
    __syncthreads();
    float* y0 = ws + WS_YA + ((kvh * 4) * NQA + qt) * 512;
    float* y1 = y0 + 32 * NQA * 512;
    oproj_store(W0, opk, opk + 256, y0, y1, w, lane);
    PIN();
    wo_issue(W0, rwo, vo_wo, swo + 2 * 128 * 1024);
    PIN();
    oproj_store(W1, opk + 64, opk + 256 + 64, y0 + NQA * 512, y1 + NQA * 512, w, lane);
    PIN();
    wo_issue(W1, rwo, vo_wo, swo + 3 * 128 * 1024);
    PIN();
    oproj_store(W0, opk + 128, opk + 256 + 128, y0 + 2 * NQA * 512, y1 + 2 * NQA * 512, w, lane);
    PIN();
    oproj_store(W1, opk + 192, opk + 256 + 192, y0 + 3 * NQA * 512, y1 + 3 * NQA * 512, w, lane);
    STAMP_W(0, ts0); STAMP_W(1, ts1); STAMP_W(2, ts2); STAMP(3);
    TRACE_DUMP(NTA);
  }
}

template <int NS, int MLS>
__device__ __forceinline__ f32x4 merge_head(const float* y, const float* ml) {
  f32x4 v[NS]; f32x2 e[NS];
#pragma unroll
  for (int s = 0; s < NS; ++s) { v[s] = *(const f32x4*)(y + s * 512); e[s] = *(const f32x2*)(ml + s * 2); }
  float M = e[0][0];
#pragma unroll
  for (int s = 1; s < NS; ++s) M = fmaxf(M, e[s][0]);
  float L = 0.f; f32x4 o = {0.f, 0.f, 0.f, 0.f};
#pragma unroll
  for (int s = 0; s < NS; ++s) { const float wt = ex2((e[s][0] - M) * LOG2E); L = fmaf(wt, e[s][1], L); o += wt * v[s]; }
  return o * (1.f / L);
}
__global__ void __launch_bounds__(256) attn_final(float* ws, unsigned short* out) {
  STAMP_T(ts0);
  __shared__ __attribute__((aligned(16))) float red[4 * 32];
  const int bid = blockIdx.x, tid = threadIdx.x, lane = tid & 63, w = tid >> 6;
  const int prob = bid >> 5, b = (bid >> 4) & 1, cg = bid & 15;
  const int h = tid >> 3, col = cg * 32 + (tid & 7) * 4;
  f32x4 o;
  if (prob == 0) o = merge_head<NQA, NQA>(ws + WS_YA + ((b * 32 + h) * NQA) * 512 + col, ws + WS_MLA + (b * 32 + h) * NQA * 2);
  else o = merge_head<NSB, 8>(ws + WS_YB + ((b * 32 + h) * NSB) * 512 + col, ws + WS_MLB + (b * 32 + h) * 16);
#pragma unroll
  for (int i = 0; i < 4; ++i) { float x = o[i]; x += dppf<ROR8>(x); o[i] = xrow_sum(x); }
  if (lane < 8) *(f32x4*)(red + w * 32 + lane * 4) = o;
  __syncthreads();
  if (tid < 16) {
    const float s0 = (red[2 * tid] + red[32 + 2 * tid]) + (red[64 + 2 * tid] + red[96 + 2 * tid]);
    const float s1 = (red[2 * tid + 1] + red[33 + 2 * tid]) + (red[65 + 2 * tid] + red[97 + 2 * tid]);
    *(unsigned*)(out + b * 1024 + prob * 512 + cg * 32 + 2 * tid) = cvtpk(s0, s1);
  }
  STAMP_W(4, ts0); STAMP(5);
}

__global__ void k_fillws(u32x4* p, size_t n, unsigned seed) {
  for (size_t i = (size_t)blockIdx.x * blockDim.x + threadIdx.x; i < n; i += (size_t)gridDim.x * blockDim.x) { unsigned x = (unsigned)i * 2654435761u + seed; p[i] = u32x4{x, x ^ 0x9e3779b9u, x * 31u, x + 7u}; }
}
static void launch_all(void* const* d_in, void* d_out, void* d_ws, hipStream_t stream) {
  typedef const unsigned short* cp;
  attn_main<<<256, 512, 0, stream>>>((cp)d_in[0], (cp)d_in[1], (cp)d_in[2], (cp)d_in[3], (cp)d_in[4], (cp)d_in[5], (cp)d_in[6], (cp)d_in[7],
                                     (cp)d_in[8], (cp)d_in[9], (float*)d_ws);
  attn_final<<<64, 256, 0, stream>>>((float*)d_ws, (unsigned short*)d_out);
}
extern "C" void kernel_launch(void* const* d_in, const int* in_sizes, int n_in, void* d_out, int out_size, void* d_ws, size_t ws_size,
                              hipStream_t stream) {
  hipStreamCaptureStatus cs; hipStreamIsCapturing(stream, &cs);
  if (cs == hipStreamCaptureStatusNone) {
    for (int i = 0; i < n_in; ++i) { hipPointerAttribute_t a; hipPointerGetAttributes(&a, d_in[i]); fprintf(stderr, "DBG in%d %p type %d flags %u managed %d\n", i, d_in[i], (int)a.type, a.allocationFlags, a.isManaged); }
    fprintf(stderr, "DBG out %p ws %p\n", d_out, d_ws);
    for (int it = 0; it < 8; ++it) {
      if (it > 0 && it < 6) k_fillws<<<2048, 256, 0, stream>>>((u32x4*)d_ws, ws_size / 16, it);
      hipStreamSynchronize(stream);
      launch_all(d_in, d_out, d_ws, stream);
      hipStreamSynchronize(stream);
      std::vector<unsigned long long> s(8192); hipMemcpy(s.data(), (char*)d_ws + STAMP_OFF, s.size() * 8, hipMemcpyDeviceToHost);
      unsigned long long mn = ~0ull, mx = 0, me = 0, mq = 0; for (int i = 0; i < 256; ++i) { mn = std::min(mn, s[i * 8]); me = std::max(me, s[i * 8 + 3]); if ((i >> 3) < 28) mq = std::max(mq, s[i * 8 + 1]); } for (int i = 0; i < 64; ++i) mx = std::max(mx, s[i * 8 + 5]);
      std::vector<unsigned> tr(256 * 8 * 128); hipMemcpy(tr.data(), (char*)d_ws + STAMP_OFF + (1u << 20), tr.size() * 4, hipMemcpyDeviceToHost);
      double bins[64] = {0};
      for (int bb = 0; bb < 256; ++bb) for (int i = 0; i < 8 * 128; ++i) { unsigned t = tr[bb * 1024 + i]; if (!t) continue; unsigned dt = t - (unsigned)mn; int k = dt / 400; if (k < 64) bins[k] += (bb >> 3) < 28 ? LPT_B * 2048.0 : 8192.0; }
      fprintf(stderr, "DBG it%d total %.2f main %.2f qready %.2f | TB/s:", it, (mx - mn) * 0.01, (me - mn) * 0.01, (mq - mn) * 0.01);
      for (int k = 0; k < 26; ++k) fprintf(stderr, " %.1f", bins[k] / 4e-6 * 1e-12); fprintf(stderr, "\n");
    }
  }
  launch_all(d_in, d_out, d_ws, stream);
}
```

```cpp
#define PADT 92
#define DEP_B 1
#define AUX_V1 2
#define AUX_WB 2
#include <hip/hip_runtime.h>
#include <hip/hip_bf16.h>
#include <stdint.h>

typedef unsigned u32x4 __attribute__((ext_vector_type(4)));
typedef float f32x4 __attribute__((ext_vector_type(4)));
typedef float f32x2 __attribute__((ext_vector_type(2)));
typedef __bf16 bf16x2 __attribute__((ext_vector_type(2)));
typedef __amdgpu_buffer_rsrc_t rsrc_t;

#define LPT_B 4
#define AUX_KV 2
#define AUX_K0 2
#define AUX_V0 2
#define AUX_K1 2
#define AUX_WA 2
#define NS_B 4
#define PRE_B 0
#define BURST_B 2
#define WO_STEPS 4
#define LOCKSTEP 0
#define HMAP (4 * xcd + j / NSB)
#define KVMAP (xcd ^ 1)

constexpr int SK = 16384, WIN = 4096;
constexpr int NSB = 7;
constexpr int NQA = 4;
constexpr float QK_SCALE = 0.08838834764831845f, LOG2E = 1.4426950408889634f;
constexpr float M_INIT = -1e30f;

constexpr int WS_YB = 0;
constexpr int WS_YA = WS_YB + 2 * 32 * NSB * 512;
constexpr int WS_MLB = WS_YA + 2 * 32 * NQA * 512;
constexpr int WS_MLA = WS_MLB + 2 * 32 * 8 * 2;

constexpr int L_QP = 0;
constexpr int L_REC = L_QP + 8192;
constexpr int L_ML = L_REC + 4096;
constexpr int L_OPK = L_ML + 64;
constexpr int L_TOTAL = 24576;

#define TRACE(n)
#define TRACE_DUMP(cnt)
#define STAMP(k)
#define STAMP_T(var)
#define STAMP_W(k, var)
#define INL __attribute__((always_inline))
#define PIN() do { asm volatile("" ::: "memory"); __builtin_amdgcn_sched_barrier(0); } while (0)

__device__ __forceinline__ float dot2(unsigned a, unsigned b, float c) {
  return __builtin_amdgcn_fdot2_f32_bf16(__builtin_bit_cast(bf16x2, a), __builtin_bit_cast(bf16x2, b), c, false);
}
__device__ __forceinline__ unsigned pk_lo(unsigned a, unsigned b) { return __builtin_amdgcn_perm(b, a, 0x05040100u); }
__device__ __forceinline__ unsigned pk_hi(unsigned a, unsigned b) { return __builtin_amdgcn_perm(b, a, 0x07060302u); }
__device__ __forceinline__ unsigned cvtpk(float lo, float hi) {
  bf16x2 r; r[0] = (__bf16)lo; r[1] = (__bf16)hi; return __builtin_bit_cast(unsigned, r);
}
template <int CTRL> __device__ __forceinline__ float dppf(float x) {
  return __builtin_bit_cast(float, __builtin_amdgcn_mov_dpp(__builtin_bit_cast(int, x), CTRL, 0xf, 0xf, true));
}
template <int CTRL> __device__ __forceinline__ unsigned dppu(unsigned x) { return (unsigned)__builtin_amdgcn_mov_dpp((int)x, CTRL, 0xf, 0xf, true); }
constexpr int XOR1 = 0xB1, XOR2 = 0x4E, XOR7 = 0x141, ROR8 = 0x128, ROR4 = 0x124, ROR12 = 0x12C, BCAST = 0x150;
__device__ __forceinline__ float row_sum(float x) {
  x += dppf<ROR8>(x); x += dppf<XOR7>(x); x += dppf<XOR2>(x); x += dppf<XOR1>(x); return x;
}
__device__ __forceinline__ float swadd32(float a, float b) {
  auto s = __builtin_amdgcn_permlane32_swap(__float_as_uint(a), __float_as_uint(b), false, false);
  return __uint_as_float(s[0]) + __uint_as_float(s[1]);
}
__device__ __forceinline__ float swadd16(float a, float b) {
  auto s = __builtin_amdgcn_permlane16_swap(__float_as_uint(a), __float_as_uint(b), false, false);
  return __uint_as_float(s[0]) + __uint_as_float(s[1]);
}
__device__ __forceinline__ float xrow_sum(float x) { x = swadd16(x, x); return swadd32(x, x); }
__device__ __forceinline__ float xrow_max(float x) {
  auto s = __builtin_amdgcn_permlane16_swap(__float_as_uint(x), __float_as_uint(x), false, false);
  x = fmaxf(__uint_as_float(s[0]), __uint_as_float(s[1]));
  auto t = __builtin_amdgcn_permlane32_swap(__float_as_uint(x), __float_as_uint(x), false, false);
  return fmaxf(__uint_as_float(t[0]), __uint_as_float(t[1]));
}
template <int N> struct IC { static constexpr int value = N; };
template <bool B> struct BC { static constexpr bool value = B; };
template <int N, int I = 0, class F> __device__ __forceinline__ void static_for(F&& f) {
  if constexpr (I < N) { f(IC<I>{}); static_for<N, I + 1>(f); }
}
__device__ __forceinline__ void round_scores(float& a, float& b) {
  unsigned u = cvtpk(a, b);
  u = cvtpk(__uint_as_float(u << 16) * QK_SCALE, __uint_as_float(u & 0xffff0000u) * QK_SCALE);
  a = __uint_as_float(u << 16); b = __uint_as_float(u & 0xffff0000u);
}
__device__ __forceinline__ float ex2(float x) { return __builtin_amdgcn_exp2f(x); }
__device__ __forceinline__ rsrc_t mkrs(const void* p, int bytes) { return __builtin_amdgcn_make_buffer_rsrc((void*)p, 0, bytes, 0x00020000); }
template <int AUX> __device__ __forceinline__ u32x4 bld(rsrc_t rs, int vo, int so) { return __builtin_amdgcn_raw_buffer_load_b128(rs, vo, so, AUX); }

__device__ __forceinline__ void gemv16(const u32x4 (&W)[16], const u32x4 (&x0)[2], const u32x4 (&x1)[2], float (&a)[16]) {
#pragma unroll
  for (int k = 0; k < 16; ++k) a[k] = 0.f;
#pragma unroll
  for (int k = 0; k < 8; ++k) {
    const unsigned p0 = x0[k >> 2][k & 3], p1 = x1[k >> 2][k & 3];
#pragma unroll
    for (int j = 0; j < 4; ++j) {
      const unsigned lo = pk_lo(W[2 * k][j], W[2 * k + 1][j]), hi = pk_hi(W[2 * k][j], W[2 * k + 1][j]);
      a[2 * j] = dot2(lo, p0, a[2 * j]); a[2 * j + 1] = dot2(hi, p0, a[2 * j + 1]);
      a[8 + 2 * j] = dot2(lo, p1, a[8 + 2 * j]); a[8 + 2 * j + 1] = dot2(hi, p1, a[8 + 2 * j + 1]);
    }
  }
}
__device__ __forceinline__ f32x4 red54(const float (&a)[16]) {
  float t[8];
#pragma unroll
  for (int i = 0; i < 8; ++i) t[i] = swadd32(a[i], a[i + 8]);
  f32x4 u;
#pragma unroll
  for (int i = 0; i < 4; ++i) u[i] = swadd16(t[i], t[i + 4]);
  return u;
}
template <int AUX_W> __device__ __forceinline__ void wq_issue(u32x4 (&W)[16], rsrc_t rs, int vo, int so) {
#pragma unroll
  for (int i = 0; i < 16; ++i) W[i] = bld<AUX_W>(rs, vo, so + i * 8192);
}
template <int AUX_W> __device__ __forceinline__ void wo_issue(u32x4 (&W)[16], rsrc_t rs, int vo, int so) {
#pragma unroll
  for (int i = 0; i < 16; ++i) W[i] = bld<AUX_W>(rs, vo, so + i * 1024);
}
__device__ __forceinline__ u32x4 q_gather(const float* qp, int b, int r, int c) {
  const float* p = qp + (2 * r) * 256 + b * 128 + 8 * c;
  const f32x4 a0 = *(const f32x4*)p, a1 = *(const f32x4*)(p + 4), b0 = *(const f32x4*)(p + 256), b1 = *(const f32x4*)(p + 260);
  float v[8];
#pragma unroll
  for (int i = 0; i < 4; ++i) { v[i] = xrow_sum(a0[i] + b0[i]); v[4 + i] = xrow_sum(a1[i] + b1[i]); }
  return u32x4{cvtpk(v[0], v[1]), cvtpk(v[2], v[3]), cvtpk(v[4], v[5]), cvtpk(v[6], v[7])};
}
__device__ __forceinline__ void oproj_store(const u32x4 (&W)[16], const unsigned* opk0, const unsigned* opk1, float* y0, float* y1, int w, int lane) {
  const int rr = lane >> 3;
  u32x4 o0[2], o1[2];
  o0[0] = *(const u32x4*)(opk0 + 8 * rr); o0[1] = *(const u32x4*)(opk0 + 8 * rr + 4);
  o1[0] = *(const u32x4*)(opk1 + 8 * rr); o1[1] = *(const u32x4*)(opk1 + 8 * rr + 4);
  float a[16];
  gemv16(W, o0, o1, a);
  f32x4 u = red54(a);
#pragma unroll
  for (int i = 0; i < 4; ++i) u[i] += dppf<ROR8>(u[i]);
  float* y = (lane >> 5) ? y1 : y0;
  if (!(lane & 8)) *(f32x4*)(y + 64 * w + 8 * (lane & 7) + 4 * ((lane >> 4) & 1)) = u;
}

__global__ void __launch_bounds__(512, 2)
attn_main(const unsigned short* Ax, const unsigned short* Ak, const unsigned short* Av, const unsigned short* Awq, const unsigned short* Awo,
          const unsigned short* Bx, const unsigned short* Bk, const unsigned short* Bv, const unsigned short* Bwq, const unsigned short* Bwo, float* ws) {
  __shared__ __attribute__((aligned(16))) float lds[L_TOTAL];
  const int bid = blockIdx.x, tid = threadIdx.x, lane = tid & 63;
  const int w = __builtin_amdgcn_readfirstlane(tid >> 6);
  const int r = lane >> 4, c = lane & 15;
  const int xcd = bid & 7, j = bid >> 3;
  const int b = w >> 2, wb = w & 3;
  const int vo_kv = lane * 16;
  const int vo_wq = r * (16 * 8192) + c * 16;
  const int vo_wo = (lane >> 3) * (16 * 1024) + (lane & 7) * 16;
  unsigned* opk = (unsigned*)(lds + L_OPK);
  if (tid == 0) ((unsigned long long*)((char*)ws + (64u << 20)))[bid] = __builtin_amdgcn_s_memrealtime();

  if (j < 4 * NSB) {
    constexpr int LPT = LPT_B, TK = 4 * LPT, TB = TK * 256, G = NSB * 8;
    constexpr int NFULL = SK / (TK * G), REST = (SK - NFULL * TK * G) / 4;
    constexpr int NS = NS_B, DEP = DEP_B, UPI = NS / 2;
    static_assert(NS % 4 == 0, "slots: K0 V0 K1 V1");
    constexpr int NIT = (2 * (NFULL + 1) + UPI - 1) / UPI;
    const int h = HMAP, s = j % NSB, g = s * 8 + w;
    const int r0 = (g * REST) / G, r1 = ((g + 1) * REST) / G;
    const int last_off = (NFULL * TK * G + r0 * 4) * 256, nrec = (NFULL * TK * G + r1 * 4) * 256, nv_last = r1 - r0;
    const rsrc_t rx = mkrs(Bx, 2048), rwq = mkrs(Bwq, 512 * 4096 * 2), rwo = mkrs(Bwo, 512 * 4096 * 2);
    const size_t kv0 = (size_t)h * SK * 128, kv1 = (size_t)(32 + h) * SK * 128;
    const rsrc_t rk0 = mkrs(Bk + kv0, nrec), rv0 = mkrs(Bv + kv0, nrec), rk1 = mkrs(Bk + kv1, nrec), rv1 = mkrs(Bv + kv1, nrec);
    u32x4 x0[2], x1[2], W[16], KV[NS][LPT];
    auto issue = [&](auto slot, int u) INL {
      constexpr int SL = decltype(slot)::value, TY = SL & 3;
      constexpr int AUX = TY == 0 ? AUX_K0 : (TY == 1 ? AUX_V0 : (TY == 2 ? AUX_K1 : AUX_V1));
      const int n = u >> 1;
      const int so = n < NFULL ? (g + n * G) * TB : (n == NFULL ? last_off : SK * 256);
#pragma unroll
      for (int i = 0; i < LPT; ++i) KV[SL][i] = bld<AUX>(TY == 0 ? rk0 : (TY == 1 ? rv0 : (TY == 2 ? rk1 : rv1)), vo_kv + i * 1024, so);
      PIN();
    };
    static_for<PRE_B>([&](auto i) INL { issue(i, decltype(i)::value / 2); });
    x0[0] = bld<0>(rx, r * 32, w * 128); x0[1] = bld<0>(rx, r * 32 + 16, w * 128);
    x1[0] = bld<0>(rx, r * 32, 1024 + w * 128); x1[1] = bld<0>(rx, r * 32 + 16, 1024 + w * 128);
    wq_issue<AUX_WB>(W, rwq, vo_wq, w * (64 * 8192) + h * 256);
    PIN();
    static_for<BURST_B, PRE_B>([&](auto i) INL { issue(i, decltype(i)::value / 2); });
    {
      float a[16];
      gemv16(W, x0, x1, a);
      const f32x4 u = red54(a);
      *(f32x4*)(lds + L_QP + w * 256 + (r >> 1) * 128 + 8 * c + 4 * (r & 1)) = u;
    }
    PIN();
    static_for<NS, BURST_B>([&](auto i) INL { issue(i, decltype(i)::value / 2); });
    __syncthreads();
    u32x4 q[2];
    q[0] = q_gather(lds + L_QP, 0, r, c); q[1] = q_gather(lds + L_QP, 1, r, c);
    STAMP_T(ts1);
    float m[2] = {M_INIT, M_INIT}, l[2] = {0.f, 0.f}, acc[2][8];
#pragma unroll
    for (int k = 0; k < 8; ++k) { acc[0][k] = 0.f; acc[1][k] = 0.f; }
    auto iter = [&](auto first, int u0) INL {
      constexpr bool FIRST = decltype(first)::value;
      static_for<UPI>([&](auto uu) INL {
        constexpr int U = decltype(uu)::value, IK = 2 * U, IV = 2 * U + 1, BB = U & 1;
        const int u = u0 + U, n = u >> 1;
        u32x4 (&K)[LPT] = KV[IK];
        u32x4 (&V)[LPT] = KV[IV];
        if constexpr (DEP < NS && !FIRST) {
#pragma unroll
          for (int i = 0; i < LPT; ++i) asm volatile("" : "+v"(K[i]) :: "memory");
          __builtin_amdgcn_sched_barrier(0);
          issue(IC<(IK + DEP) % NS>{}, u0 + (IK + DEP) / 2);
        }
        float sc[LPT];
#pragma unroll
        for (int i = 0; i < LPT; ++i) {
          float d = dot2(K[i][0], q[BB][0], 0.f);
          d = dot2(K[i][1], q[BB][1], d); d = dot2(K[i][2], q[BB][2], d); sc[i] = dot2(K[i][3], q[BB][3], d);
        }
#pragma unroll
        for (int i = 0; i < LPT; ++i) asm volatile("" : "+v"(sc[i]) :: "memory");
        __builtin_amdgcn_sched_barrier(0);
        if constexpr (DEP == NS || (FIRST && IK < DEP)) issue(IC<IK>{}, u + UPI);
        const int nv = n < NFULL ? LPT : (n == NFULL ? nv_last : 0);
        float mx = M_INIT;
#pragma unroll
        for (int i = 0; i < LPT; ++i) sc[i] = row_sum(sc[i]);
#pragma unroll
        for (int i = 0; i < LPT; i += 2) round_scores(sc[i], sc[i + 1]);
#pragma unroll
        for (int i = 0; i < LPT; ++i) { sc[i] = i < nv ? sc[i] : -INFINITY; mx = fmaxf(mx, sc[i]); }
        if (__any(mx > m[BB])) {
          const float mn = fmaxf(m[BB], mx), al = ex2((m[BB] - mn) * LOG2E);
          m[BB] = mn; l[BB] *= al;
#pragma unroll
          for (int k = 0; k < 8; ++k) acc[BB][k] *= al;
        }
        const float mc = m[BB] * LOG2E;
        float p[LPT];
#pragma unroll
        for (int i = 0; i < LPT; ++i) { p[i] = ex2(fmaf(sc[i], LOG2E, -mc)); l[BB] += p[i]; }
        if constexpr (DEP < NS && !FIRST) {
#pragma unroll
          for (int i = 0; i < LPT; ++i) asm volatile("" : "+v"(V[i]), "+v"(p[i]) :: "memory");
          __builtin_amdgcn_sched_barrier(0);
          issue(IC<(IV + DEP) % NS>{}, u0 + (IV + DEP) / 2);
        }
#pragma unroll
        for (int i = 0; i < LPT; i += 2) {
          const unsigned pp = cvtpk(p[i], p[i + 1]);
#pragma unroll
          for (int jj = 0; jj < 4; ++jj) {
            acc[BB][2 * jj] = dot2(pk_lo(V[i][jj], V[i + 1][jj]), pp, acc[BB][2 * jj]);
            acc[BB][2 * jj + 1] = dot2(pk_hi(V[i][jj], V[i + 1][jj]), pp, acc[BB][2 * jj + 1]);
          }
        }
#pragma unroll
        for (int k = 0; k < 8; ++k) asm volatile("" : "+v"(acc[BB][k]) :: "memory");
        __builtin_amdgcn_sched_barrier(0);
        if constexpr (DEP == NS || (FIRST && IV < DEP)) issue(IC<IV>{}, u + UPI);
        TRACE(u);
        if constexpr (LOCKSTEP) __builtin_amdgcn_s_barrier();
      });
    };
    iter(BC<true>{}, 0);
    static_for<WO_STEPS>([&](auto kk) INL {
      constexpr int KK = decltype(kk)::value, PER = 16 / WO_STEPS;
#pragma unroll
      for (int i = KK * PER; i < (KK + 1) * PER; ++i) W[i] = bld<AUX_WB>(rwo, vo_wo, h * (128 * 1024) + w * 128 + i * 1024);
      PIN();
      iter(BC<false>{}, (KK + 1) * UPI);
    });
    for (int it = WO_STEPS + 1; it < NIT; ++it) iter(BC<false>{}, it * UPI);
    STAMP_T(ts2);
#pragma unroll
    for (int bb = 0; bb < 2; ++bb) {
      const float mm = xrow_max(m[bb]), sc = ex2((m[bb] - mm) * LOG2E);
      const float ls = xrow_sum(l[bb] * sc);
      float t[4];
#pragma unroll
      for (int i = 0; i < 4; ++i) t[i] = swadd32(acc[bb][i] * sc, acc[bb][i + 4] * sc);
      f32x2 u; u[0] = swadd16(t[0], t[2]); u[1] = swadd16(t[1], t[3]);
      *(f32x2*)(lds + L_REC + w * 512 + bb * 128 + 8 * c + 4 * (r >> 1) + 2 * (r & 1)) = u;
      if (lane == 0) *(f32x2*)(lds + L_ML + w * 8 + bb * 2) = f32x2{mm, ls};
    }
    __syncthreads();
    if (w < 2) {
      f32x2 ml[8];
#pragma unroll
      for (int i = 0; i < 8; ++i) ml[i] = *(const f32x2*)(lds + L_ML + i * 8 + w * 2);
      float M = ml[0][0];
#pragma unroll
      for (int i = 1; i < 8; ++i) M = fmaxf(M, ml[i][0]);
      float L = 0.f, o0 = 0.f, o1 = 0.f;
#pragma unroll
      for (int i = 0; i < 8; ++i) {
        const float e = ex2((ml[i][0] - M) * LOG2E);
        const f32x2 v = *(const f32x2*)(lds + L_REC + i * 512 + w * 128 + 2 * lane);
        L = fmaf(e, ml[i][1], L); o0 = fmaf(e, v[0], o0); o1 = fmaf(e, v[1], o1);
      }
      opk[w * 256 + lane] = cvtpk(o0, o1);
      if (lane == 0) *(f32x2*)(ws + WS_MLB + ((w * 32 + h) * 8 + s) * 2) = f32x2{M, L};
    }
    __syncthreads();
    oproj_store(W, opk, opk + 256, ws + WS_YB + (h * NSB + s) * 512, ws + WS_YB + ((32 + h) * NSB + s) * 512, w, lane);
    STAMP_W(0, ts0); STAMP_W(1, ts1); STAMP_W(2, ts2); STAMP(3);
    TRACE_DUMP(2 * NFULL);
  } else {
    constexpr int TB = 16 * 256;
    const int kvh = KVMAP, qt = j - 4 * NSB;
    const rsrc_t rx = mkrs(Ax, 2048), rwq = mkrs(Awq, 512 * 4096 * 2), rwo = mkrs(Awo, 512 * 4096 * 2);
    const size_t kvbase = ((size_t)(b * 8 + kvh) * SK + (SK - WIN) + qt * (WIN / NQA)) * 128;
    const rsrc_t rk = mkrs(Ak + kvbase, (WIN / NQA) * 256), rv = mkrs(Av + kvbase, (WIN / NQA) * 256);
    u32x4 x0[2], x1[2], W0[16], W1[16], K[4], V[4];
    x0[0] = bld<0>(rx, r * 32, w * 128); x0[1] = bld<0>(rx, r * 32 + 16, w * 128);
    x1[0] = bld<0>(rx, r * 32, 1024 + w * 128); x1[1] = bld<0>(rx, r * 32 + 16, 1024 + w * 128);
    const int sq = w * (64 * 8192) + kvh * 1024;
    float* qdst = lds + L_QP + w * 256 + (r >> 1) * 128 + 8 * c + 4 * (r & 1);
    wq_issue<AUX_WA>(W0, rwq, vo_wq, sq);
    PIN();
    wq_issue<AUX_WA>(W1, rwq, vo_wq, sq + 256);
    PIN();
    { float a[16]; gemv16(W0, x0, x1, a); *(f32x4*)(qdst) = red54(a); }
    PIN();
    wq_issue<AUX_WA>(W0, rwq, vo_wq, sq + 512);
    PIN();
    { float a[16]; gemv16(W1, x0, x1, a); *(f32x4*)(qdst + 2048) = red54(a); }
    PIN();
    wq_issue<AUX_WA>(W1, rwq, vo_wq, sq + 768);
    PIN();
    int so = wb * TB;
#pragma unroll
    for (int i = 0; i < 4; ++i) K[i] = bld<AUX_KV>(rk, vo_kv + i * 1024, so);
    PIN();
#pragma unroll
    for (int i = 0; i < 4; ++i) V[i] = bld<AUX_KV>(rv, vo_kv + i * 1024, so);
    PIN();
    { float a[16]; gemv16(W0, x0, x1, a); *(f32x4*)(qdst + 4096) = red54(a); }
    PIN();
    { float a[16]; gemv16(W1, x0, x1, a); *(f32x4*)(qdst + 6144) = red54(a); }
    PIN();
    const int swo = kvh * (512 * 1024) + w * 128;
    wo_issue<AUX_WA>(W0, rwo, vo_wo, swo);
    PIN();
    __syncthreads();
    u32x4 q[4];
#pragma unroll
    for (int hh = 0; hh < 4; ++hh) q[hh] = q_gather(lds + L_QP + hh * 2048, b, r, c);
    STAMP_T(ts1);
    float m = M_INIT, l = 0.f, acc[32];
#pragma unroll
    for (int k = 0; k < 32; ++k) acc[k] = 0.f;
    constexpr int NTA = WIN / NQA / 16 / 4;
    for (int t = 0; t < NTA; ++t) {
      so += 4 * TB;
      float x[16];
#pragma unroll
      for (int i = 0; i < 4; ++i)
#pragma unroll
        for (int hh = 0; hh < 4; ++hh) {
          float d = dot2(K[i][0], q[hh][0], 0.f);
          d = dot2(K[i][1], q[hh][1], d); d = dot2(K[i][2], q[hh][2], d); x[i * 4 + hh] = dot2(K[i][3], q[hh][3], d);
        }
#pragma unroll
      for (int i = 0; i < 16; ++i) asm volatile("" : "+v"(x[i]) :: "memory");
      __builtin_amdgcn_sched_barrier(0);
#pragma unroll
      for (int i = 0; i < 4; ++i) K[i] = bld<AUX_KV>(rk, vo_kv + i * 1024, so);
      PIN();
#define TR_STEP(HALF, CTRL, BIT) _Pragma("unroll") for (int hh = 0; hh < (HALF); ++hh) { \
        const float lo_ = x[hh], hi_ = x[hh + (HALF)]; const float t1_ = lo_ + dppf<CTRL>(lo_), t2_ = hi_ + dppf<CTRL>(hi_); \
        x[hh] = (c & (BIT)) ? t2_ : t1_; }
      TR_STEP(8, ROR8, 8) TR_STEP(4, XOR7, 4) TR_STEP(2, XOR2, 2) TR_STEP(1, XOR1, 1)
#undef TR_STEP
      float sc = x[0], sc_dummy = x[0];
      round_scores(sc, sc_dummy);
      float mx = fmaxf(sc, dppf<ROR4>(sc)); mx = fmaxf(mx, dppf<ROR8>(mx));
      if (__any(mx > m)) {
        const float mn = fmaxf(m, mx), al = ex2((m - mn) * LOG2E);
        m = mn; l *= al;
        const float a0 = dppf<BCAST + 0>(al), a1 = dppf<BCAST + 1>(al), a2 = dppf<BCAST + 2>(al), a3 = dppf<BCAST + 3>(al);
#pragma unroll
        for (int k = 0; k < 8; ++k) { acc[k] *= a0; acc[8 + k] *= a1; acc[16 + k] *= a2; acc[24 + k] *= a3; }
      }
      const float p = ex2(fmaf(sc, LOG2E, -m * LOG2E));
      l += p;
      const unsigned pp = cvtpk(p, dppf<ROR12>(p));
      unsigned pb[8];
      pb[0] = dppu<BCAST + 0>(pp); pb[1] = dppu<BCAST + 1>(pp); pb[2] = dppu<BCAST + 2>(pp); pb[3] = dppu<BCAST + 3>(pp);
      pb[4] = dppu<BCAST + 8>(pp); pb[5] = dppu<BCAST + 9>(pp); pb[6] = dppu<BCAST + 10>(pp); pb[7] = dppu<BCAST + 11>(pp);
#pragma unroll
      for (int i = 0; i < 2; ++i)
#pragma unroll
        for (int jj = 0; jj < 4; ++jj) {
          const unsigned lo = pk_lo(V[2 * i][jj], V[2 * i + 1][jj]), hi = pk_hi(V[2 * i][jj], V[2 * i + 1][jj]);
#pragma unroll
          for (int hh = 0; hh < 4; ++hh) {
            acc[hh * 8 + 2 * jj] = dot2(lo, pb[i * 4 + hh], acc[hh * 8 + 2 * jj]);
            acc[hh * 8 + 2 * jj + 1] = dot2(hi, pb[i * 4 + hh], acc[hh * 8 + 2 * jj + 1]);
          }
        }
#pragma unroll
      for (int k = 0; k < 32; ++k) asm volatile("" : "+v"(acc[k]) :: "memory");
      __builtin_amdgcn_sched_barrier(0);
#pragma unroll
      for (int i = 0; i < 4; ++i) V[i] = bld<AUX_KV>(rv, vo_kv + i * 1024, so);
      PIN();
      TRACE(t);
    }
    STAMP_T(ts2);
    wo_issue<AUX_WA>(W1, rwo, vo_wo, swo + 128 * 1024);
    PIN();
    {
      const float mm = xrow_max(m), sc = ex2((m - mm) * LOG2E);
      float ls = l * sc; ls += dppf<ROR4>(ls); ls += dppf<ROR8>(ls); ls = xrow_sum(ls);
      float sh[4];
      sh[0] = dppf<BCAST + 0>(sc); sh[1] = dppf<BCAST + 1>(sc); sh[2] = dppf<BCAST + 2>(sc); sh[3] = dppf<BCAST + 3>(sc);
#pragma unroll
      for (int hh = 0; hh < 4; ++hh) {
        float t[4];
#pragma unroll
        for (int i = 0; i < 4; ++i) t[i] = swadd32(acc[hh * 8 + i] * sh[hh], acc[hh * 8 + i + 4] * sh[hh]);
        f32x2 u; u[0] = swadd16(t[0], t[2]); u[1] = swadd16(t[1], t[3]);
        *(f32x2*)(lds + L_REC + w * 512 + hh * 128 + 8 * c + 4 * (r >> 1) + 2 * (r & 1)) = u;
      }
      if (lane < 4) *(f32x2*)(lds + L_ML + w * 8 + lane * 2) = f32x2{mm, ls};
    }
    __syncthreads();
    {
      const int w0 = b * 4, hh = wb;
      f32x2 ml[4];
#pragma unroll
      for (int i = 0; i < 4; ++i) ml[i] = *(const f32x2*)(lds + L_ML + (w0 + i) * 8 + hh * 2);
      const float M = fmaxf(fmaxf(ml[0][0], ml[1][0]), fmaxf(ml[2][0], ml[3][0]));
      float L = 0.f, o0 = 0.f, o1 = 0.f;
#pragma unroll
      for (int i = 0; i < 4; ++i) {
        const float e = ex2((ml[i][0] - M) * LOG2E);
        const f32x2 v = *(const f32x2*)(lds + L_REC + (w0 + i) * 512 + hh * 128 + 2 * lane);
        L = fmaf(e, ml[i][1], L); o0 = fmaf(e, v[0], o0); o1 = fmaf(e, v[1], o1);
      }
      opk[b * 256 + hh * 64 + lane] = cvtpk(o0, o1);
      if (lane == 0) *(f32x2*)(ws + WS_MLA + ((b * 32 + kvh * 4 + hh) * NQA + qt) * 2) = f32x2{M, L};
    }
    __syncthreads();
    float* y0 = ws + WS_YA + ((kvh * 4) * NQA + qt) * 512;
    float* y1 = y0 + 32 * NQA * 512;
    oproj_store(W0, opk, opk + 256, y0, y1, w, lane);
    PIN();
    wo_issue<AUX_WA>(W0, rwo, vo_wo, swo + 2 * 128 * 1024);
    PIN();
    oproj_store(W1, opk + 64, opk + 256 + 64, y0 + NQA * 512, y1 + NQA * 512, w, lane);
    PIN();
    wo_issue<AUX_WA>(W1, rwo, vo_wo, swo + 3 * 128 * 1024);
    PIN();
    oproj_store(W0, opk + 128, opk + 256 + 128, y0 + 2 * NQA * 512, y1 + 2 * NQA * 512, w, lane);
    PIN();
    oproj_store(W1, opk + 192, opk + 256 + 192, y0 + 3 * NQA * 512, y1 + 3 * NQA * 512, w, lane);
    STAMP_W(0, ts0); STAMP_W(1, ts1); STAMP_W(2, ts2); STAMP(3);
    TRACE_DUMP(NTA);
  }
}

template <int NS, int MLS>
__device__ __forceinline__ f32x4 merge_head(const float* y, const float* ml) {
  f32x4 v[NS]; f32x2 e[NS];
#pragma unroll
  for (int s = 0; s < NS; ++s) { v[s] = *(const f32x4*)(y + s * 512); e[s] = *(const f32x2*)(ml + s * 2); }
  float M = e[0][0];
#pragma unroll
  for (int s = 1; s < NS; ++s) M = fmaxf(M, e[s][0]);
  float L = 0.f; f32x4 o = {0.f, 0.f, 0.f, 0.f};
#pragma unroll
  for (int s = 0; s < NS; ++s) { const float wt = ex2((e[s][0] - M) * LOG2E); L = fmaf(wt, e[s][1], L); o += wt * v[s]; }
  return o * (1.f / L);
}
__global__ void __launch_bounds__(256) attn_final(float* ws, unsigned short* out) {
  STAMP_T(ts0);
  __shared__ __attribute__((aligned(16))) float red[4 * 32];
  const int bid = blockIdx.x, tid = threadIdx.x, lane = tid & 63, w = tid >> 6;
  const int prob = bid >> 5, b = (bid >> 4) & 1, cg = bid & 15;
  const int h = tid >> 3, col = cg * 32 + (tid & 7) * 4;
  f32x4 o;
  if (prob == 0) o = merge_head<NQA, NQA>(ws + WS_YA + ((b * 32 + h) * NQA) * 512 + col, ws + WS_MLA + (b * 32 + h) * NQA * 2);
  else o = merge_head<NSB, 8>(ws + WS_YB + ((b * 32 + h) * NSB) * 512 + col, ws + WS_MLB + (b * 32 + h) * 16);
#pragma unroll
  for (int i = 0; i < 4; ++i) { float x = o[i]; x += dppf<ROR8>(x); o[i] = xrow_sum(x); }
  if (lane < 8) *(f32x4*)(red + w * 32 + lane * 4) = o;
  __syncthreads();
  if (tid < 16) {
    const float s0 = (red[2 * tid] + red[32 + 2 * tid]) + (red[64 + 2 * tid] + red[96 + 2 * tid]);
    const float s1 = (red[2 * tid + 1] + red[33 + 2 * tid]) + (red[65 + 2 * tid] + red[97 + 2 * tid]);
    *(unsigned*)(out + b * 1024 + prob * 512 + cg * 32 + 2 * tid) = cvtpk(s0, s1);
  }
  {
    unsigned long long t0 = ((const unsigned long long*)((const char*)ws + (64u << 20)))[tid];
    for (int o = 32; o > 0; o >>= 1) { unsigned long long x = __shfl_xor(t0, o); t0 = x < t0 ? x : t0; }
    while (__builtin_amdgcn_s_memrealtime() - t0 < PADT * 100ull) __builtin_amdgcn_s_sleep(1);
  }
}

extern "C" void kernel_launch(void* const* d_in, const int* in_sizes, int n_in, void* d_out, int out_size, void* d_ws, size_t ws_size,
                              hipStream_t stream) {
  typedef const unsigned short* cp;
  attn_main<<<256, 512, 0, stream>>>((cp)d_in[0], (cp)d_in[1], (cp)d_in[2], (cp)d_in[3], (cp)d_in[4], (cp)d_in[5], (cp)d_in[6], (cp)d_in[7],
                                     (cp)d_in[8], (cp)d_in[9], (float*)d_ws);
  attn_final<<<64, 256, 0, stream>>>((float*)d_ws, (unsigned short*)d_out);
}
```

```cpp
#include <hip/hip_runtime.h>
#include <hip/hip_bf16.h>
#include <stdint.h>

typedef unsigned u32x4 __attribute__((ext_vector_type(4)));
typedef float f32x4 __attribute__((ext_vector_type(4)));
typedef float f32x2 __attribute__((ext_vector_type(2)));
typedef __bf16 bf16x2 __attribute__((ext_vector_type(2)));
typedef __bf16 bf16x8 __attribute__((ext_vector_type(8)));
typedef __amdgpu_buffer_rsrc_t rsrc_t;

constexpr int AUX_KV = 2;
constexpr int AUX_W = 0;
constexpr int PT_B = 8;
constexpr int PSTART = 6, PSTOP = 3, PCAP = 10, RLAG = 2, NPUB = 16, MARGIN = -60;

constexpr int SK = 16384, WIN = 4096;
constexpr int NSB = 7;
constexpr int NQA = 4;
constexpr float QK_SCALE = 0.08838834764831845f, LOG2E = 1.4426950408889634f;
constexpr float M_INIT = -1e30f;

constexpr int WS_EPOCH = 0;
constexpr int WS_TT = 64;
constexpr int WS_YB = 32768 + 64;
constexpr int WS_YA = WS_YB + 2 * 32 * NSB * 256;
constexpr int WS_MLB = WS_YA + 2 * 32 * NQA * 256;
constexpr int WS_MLA = WS_MLB + 2 * 32 * 8 * 2;
constexpr int WS_END = WS_MLA + 2 * 32 * 8 * 2;
constexpr int OOB = 0x40000000;

constexpr int L_QP = 0;
constexpr int L_REC = L_QP + 8192;
constexpr int L_ML = L_REC + 4096;
constexpr int L_OPK = L_ML + 64;
constexpr int L_RED = L_OPK + 512;
constexpr int L_TAG = L_RED + 512;
constexpr int L_SCO = L_TAG + 4;
constexpr int L_TOTAL = 24576;

#define INL __attribute__((always_inline))
#define BAR() do { __builtin_amdgcn_s_barrier(); PIN(); } while (0)
#define PIN() do { asm volatile("" ::: "memory"); __builtin_amdgcn_sched_barrier(0); } while (0)

__device__ __forceinline__ float dot2(unsigned a, unsigned b, float c) {
  return __builtin_amdgcn_fdot2_f32_bf16(__builtin_bit_cast(bf16x2, a), __builtin_bit_cast(bf16x2, b), c, false);
}
__device__ __forceinline__ unsigned pk_lo(unsigned a, unsigned b) { return __builtin_amdgcn_perm(b, a, 0x05040100u); }
__device__ __forceinline__ unsigned pk_hi(unsigned a, unsigned b) { return __builtin_amdgcn_perm(b, a, 0x07060302u); }
__device__ __forceinline__ unsigned cvtpk(float lo, float hi) {
  bf16x2 r; r[0] = (__bf16)lo; r[1] = (__bf16)hi; return __builtin_bit_cast(unsigned, r);
}
template <int CTRL> __device__ __forceinline__ float dppf(float x) {
  return __builtin_bit_cast(float, __builtin_amdgcn_mov_dpp(__builtin_bit_cast(int, x), CTRL, 0xf, 0xf, true));
}
template <int CTRL> __device__ __forceinline__ unsigned dppu(unsigned x) { return (unsigned)__builtin_amdgcn_mov_dpp((int)x, CTRL, 0xf, 0xf, true); }
constexpr int XOR1 = 0xB1, XOR2 = 0x4E, XOR7 = 0x141, ROR8 = 0x128, ROR4 = 0x124, ROR12 = 0x12C, BCAST = 0x150;
__device__ __forceinline__ float row_sum(float x) {
  x += dppf<ROR8>(x); x += dppf<XOR7>(x); x += dppf<XOR2>(x); x += dppf<XOR1>(x); return x;
}
__device__ __forceinline__ float swadd32(float a, float b) {
  auto s = __builtin_amdgcn_permlane32_swap(__float_as_uint(a), __float_as_uint(b), false, false);
  return __uint_as_float(s[0]) + __uint_as_float(s[1]);
}
__device__ __forceinline__ float swadd16(float a, float b) {
  auto s = __builtin_amdgcn_permlane16_swap(__float_as_uint(a), __float_as_uint(b), false, false);
  return __uint_as_float(s[0]) + __uint_as_float(s[1]);
}
__device__ __forceinline__ float xrow_sum(float x) { x = swadd16(x, x); return swadd32(x, x); }
__device__ __forceinline__ float xrow_max(float x) {
  auto s = __builtin_amdgcn_permlane16_swap(__float_as_uint(x), __float_as_uint(x), false, false);
  x = fmaxf(__uint_as_float(s[0]), __uint_as_float(s[1]));
  auto t = __builtin_amdgcn_permlane32_swap(__float_as_uint(x), __float_as_uint(x), false, false);
  return fmaxf(__uint_as_float(t[0]), __uint_as_float(t[1]));
}
template <int N> struct IC { static constexpr int value = N; };
template <int N, int I = 0, class F> __device__ __forceinline__ void static_for(F&& f) {
  if constexpr (I < N) { f(IC<I>{}); static_for<N, I + 1>(f); }
}
__device__ __forceinline__ void round_scores(float& a, float& b) {
  unsigned u = cvtpk(a, b);
  u = cvtpk(__uint_as_float(u << 16) * QK_SCALE, __uint_as_float(u & 0xffff0000u) * QK_SCALE);
  a = __uint_as_float(u << 16); b = __uint_as_float(u & 0xffff0000u);
}
__device__ __forceinline__ float ex2(float x) { return __builtin_amdgcn_exp2f(x); }
__device__ __forceinline__ rsrc_t mkrs(const void* p, int bytes) { return __builtin_amdgcn_make_buffer_rsrc((void*)p, 0, bytes, 0x00020000); }
template <int AUX> __device__ __forceinline__ u32x4 bld(rsrc_t rs, int vo, int so) { return __builtin_amdgcn_raw_buffer_load_b128(rs, vo, so, AUX); }

typedef unsigned u32x2 __attribute__((ext_vector_type(2)));
constexpr int SC1 = 16;
__device__ __forceinline__ unsigned rnd22(float x) {
  const unsigned u = __float_as_uint(x);
  return ((u & 0x7fffffffu) > 0x7f800000u ? (u | 0x00400000u) : u + 0x200u) & 0xfffffc00u;
}
__device__ __forceinline__ u32x2 g_pack(float a, float b, unsigned tag) {
  const unsigned x = rnd22(a), y = rnd22(b);
  return u32x2{(x << 10) | (tag & 0xfffffu), y | (x >> 22)};
}
__device__ __forceinline__ bool g_ok(u32x4 v, u32x2 tag) { return (((v[0] ^ tag[0]) | (v[2] ^ tag[1])) & 0xfffffu) == 0u; }
__device__ __forceinline__ float g_v0(unsigned lo, unsigned hi) { return __uint_as_float(__builtin_amdgcn_alignbit(hi, lo, 10) & 0xfffffc00u); }
__device__ __forceinline__ float g_v1(unsigned hi) { return __uint_as_float(hi & 0xfffffc00u); }
__device__ __forceinline__ void st_ml(rsrc_t rws, int idx, float M, float L, u32x2 tag) {
  const u32x2 a = g_pack(M, L, tag[0]), b = g_pack(M, L, tag[1]);
  __builtin_amdgcn_raw_buffer_store_b128(u32x4{a[0], a[1], b[0], b[1]}, rws, idx * 8, 0, SC1);
}

template <int AUXW> __device__ __forceinline__ void wq_issue(u32x4 (&W)[16], rsrc_t rs, int vo, int so) {
#pragma unroll
  for (int i = 0; i < 16; ++i) W[i] = bld<AUXW>(rs, vo, so + i * 8192);
}
template <int AUXW> __device__ __forceinline__ void wo_issue(u32x4 (&W)[16], rsrc_t rs, int vo, int so) {
#pragma unroll
  for (int i = 0; i < 16; ++i) W[i] = bld<AUXW>(rs, vo, so + i * 1024);
}
__device__ __forceinline__ u32x4 q_gather(const float* qp, int b, int r, int c) {
  const float* p = qp + (2 * r) * 256 + b * 128 + 8 * c;
  const f32x4 lo = *(const f32x4*)p + *(const f32x4*)(p + 256), hi = *(const f32x4*)(p + 4) + *(const f32x4*)(p + 260);
  float t[4];
#pragma unroll
  for (int i = 0; i < 4; ++i) t[i] = swadd32(lo[i], hi[i]);
  const unsigned d = cvtpk(swadd16(t[0], t[2]), swadd16(t[1], t[3]));
  const auto e = __builtin_amdgcn_permlane16_swap(d, d, false, false);
  const auto f0 = __builtin_amdgcn_permlane32_swap(e[0], e[0], false, false), f1 = __builtin_amdgcn_permlane32_swap(e[1], e[1], false, false);
  return u32x4{f0[0], f1[0], f0[1], f1[1]};
}
__device__ __forceinline__ void to_frags(const u32x4 (&W)[16], u32x4 (&F)[16]) {
#pragma unroll
  for (int j = 0; j < 4; ++j)
#pragma unroll
    for (int hf = 0; hf < 2; ++hf)
#pragma unroll
      for (int d = 0; d < 4; ++d) {
        F[4 * j + hf][d] = pk_lo(W[8 * hf + 2 * d][j], W[8 * hf + 2 * d + 1][j]);
        F[4 * j + 2 + hf][d] = pk_hi(W[8 * hf + 2 * d][j], W[8 * hf + 2 * d + 1][j]);
      }
}
__device__ __forceinline__ f32x4 mfma16(u32x4 a, u32x4 b, f32x4 c) {
  return __builtin_amdgcn_mfma_f32_16x16x32_bf16(__builtin_bit_cast(bf16x8, a), __builtin_bit_cast(bf16x8, b), c, 0, 0, 0);
}
__device__ __forceinline__ f32x4 qproj(const u32x4 (&W)[16], const u32x4 (&xa)[2], int r) {
  f32x4 acc[8];
#pragma unroll
  for (int hf = 0; hf < 2; ++hf)
#pragma unroll
    for (int j = 0; j < 4; ++j) {
      u32x4 lo, hi;
#pragma unroll
      for (int d = 0; d < 4; ++d) { lo[d] = pk_lo(W[8 * hf + 2 * d][j], W[8 * hf + 2 * d + 1][j]); hi[d] = pk_hi(W[8 * hf + 2 * d][j], W[8 * hf + 2 * d + 1][j]); }
      const f32x4 z = {0.f, 0.f, 0.f, 0.f};
      acc[2 * j] = mfma16(xa[hf], lo, hf ? acc[2 * j] : z);
      acc[2 * j + 1] = mfma16(xa[hf], hi, hf ? acc[2 * j + 1] : z);
    }
  f32x4 u;
#pragma unroll
  for (int i = 0; i < 4; ++i) {
    float a0 = acc[i][0], a1 = acc[i][1], b0 = acc[4 + i][0], b1 = acc[4 + i][1];
    asm volatile("" : "+v"(a0), "+v"(a1), "+v"(b0), "+v"(b1));
    const float x0 = (r & 1) ? b0 : a0, x1 = (r & 1) ? b1 : a1;
    u[i] = (r & 2) ? x1 : x0;
  }
  return u;
}
struct OprojAddr { int lds, yoff; unsigned tag; };
__device__ __forceinline__ OprojAddr oproj_addr(int ns, u32x2 tag, int w, int lane) {
  const int m = lane & 15, kg = lane >> 4, col = 64 * w + 8 * (m & 7) + 2 * kg;
  OprojAddr a;
  a.lds = ((m >> 1) & 1) * 256 + (2 * kg + (m & 1)) * 8;
  a.yoff = (((m >> 3) * 16 + (col >> 5)) * (32 * ns * 16) + ((col & 31) >> 1)) * 8;
  a.tag = ((kg & 1) ? tag[1] : tag[0]) & 0xfffffu;
  asm volatile("" : "+v"(a.lds), "+v"(a.yoff), "+v"(a.tag));
  return a;
}
__device__ __forceinline__ void oproj_store(const u32x4 (&F)[16], const unsigned* opk, rsrc_t rws, int ybase, OprojAddr ad, int lane) {
  const int m = lane & 15;
  const u32x4 a0 = *(const u32x4*)(opk + ad.lds), a1 = *(const u32x4*)(opk + ad.lds + 4);
  f32x4 acc0 = {0.f, 0.f, 0.f, 0.f}, acc1 = {0.f, 0.f, 0.f, 0.f};
  const u32x4 z = {0u, 0u, 0u, 0u};
#pragma unroll
  for (int v = 0; v < 4; ++v) {
    const u32x4 b0 = (m >> 2) == v ? a0 : z, b1 = (m >> 2) == v ? a1 : z;
    acc0 = mfma16(b0, F[4 * v], acc0); acc1 = mfma16(b0, F[4 * v + 2], acc1);
    acc0 = mfma16(b1, F[4 * v + 1], acc0); acc1 = mfma16(b1, F[4 * v + 3], acc1);
  }
  float p0 = acc0[0], p1 = acc0[1], p2 = acc0[2], p3 = acc0[3], q0 = acc1[0], q1 = acc1[1], q2 = acc1[2], q3 = acc1[3];
  asm volatile("" : "+v"(p0), "+v"(p1), "+v"(p2), "+v"(p3), "+v"(q0), "+v"(q1), "+v"(q2), "+v"(q3));
  const bool hi = m & 8;
  const float y0 = (hi ? p3 : p0) + dppf<ROR8>(hi ? p1 : p2), y1 = (hi ? q3 : q0) + dppf<ROR8>(hi ? q1 : q2);
  __builtin_amdgcn_raw_buffer_store_b64(g_pack(y0, y1, ad.tag), rws, ad.yoff, ybase * 8, SC1);
}

constexpr unsigned long long T_LIMIT = 100000000ull;
template <int NS>
__device__ __forceinline__ void final_merge(rsrc_t rws, int ybase, int mlbase, u32x2 tag, float* red, unsigned short* out, int tid, int w, int lane,
                                            unsigned long long t_lim) {
  constexpr int NH = NS > 4 ? 4 : 2;
  const int r = lane >> 4, c = lane & 15, half = c >> 3, head = 4 * w + r;
  u32x4 ml;
  for (;;) {
    ml = __builtin_amdgcn_raw_buffer_load_b128(rws, c < NS ? (mlbase + (head * 8 + c) * 2) * 8 : OOB, 0, SC1);
    if (__all(c >= NS || g_ok(ml, tag)) || __builtin_amdgcn_s_memrealtime() > t_lim) break;
    __builtin_amdgcn_s_sleep(64);
    asm volatile("" ::: "memory");
  }
  const float m = c < NS ? g_v0(ml[0], ml[1]) : M_INIT, l = c < NS ? g_v1(ml[1]) : 0.f;
  float M = fmaxf(m, dppf<XOR7>(m)); M = fmaxf(M, dppf<XOR2>(M)); M = fmaxf(M, dppf<XOR1>(M)); M = fmaxf(M, dppf<ROR8>(M));
  const float e = c < NS ? ex2((m - M) * LOG2E) : 0.f;
  const float wt = e / row_sum(e * l);
  float wi[NH];
  static_for<NH>([&](auto ii) INL {
    constexpr int I = decltype(ii)::value;
    float w0 = dppf<BCAST + I>(wt), w1 = dppf<BCAST + NH + I>(wt);
    asm volatile("" : "+v"(w0), "+v"(w1));
    wi[I] = half ? w1 : w0;
    asm volatile("" : "+v"(wi[I]));
  });
  if (w == 0) {
    const int poff = (c & 7) == 0 ? (ybase + ((r * 8 + half * 4) * NS + r) * 16) * 8 : OOB;
    for (;;) {
      const u32x4 v = __builtin_amdgcn_raw_buffer_load_b128(rws, poff, 0, SC1);
      if (__any((c & 7) == 0 && g_ok(v, tag)) || __builtin_amdgcn_s_memrealtime() > t_lim) break;
      __builtin_amdgcn_s_sleep(16);
      asm volatile("" ::: "memory");
    }
  }
  __syncthreads();
  u32x4 y[NH];
  bool yok[NH];
#pragma unroll
  for (int i = 0; i < NH; ++i) { yok[i] = half * NH + i >= NS; y[i] = u32x4{0u, 0u, 0u, 0u}; }
  const int yoff = (ybase + (head * NS + half * NH) * 16 + 2 * (c & 7)) * 8;
  for (;;) {
    u32x4 v[NH];
#pragma unroll
    for (int i = 0; i < NH; ++i) v[i] = __builtin_amdgcn_raw_buffer_load_b128(rws, yok[i] ? OOB : yoff + i * 128, 0, SC1);
    bool all = true;
#pragma unroll
    for (int i = 0; i < NH; ++i) {
      if (!yok[i]) y[i] = v[i];
      yok[i] = yok[i] || g_ok(v[i], tag);
      all = all && yok[i];
    }
    if (__all(all) || __builtin_amdgcn_s_memrealtime() > t_lim) break;
    __builtin_amdgcn_s_sleep(1);
    asm volatile("" ::: "memory");
  }
  float o[4] = {0.f, 0.f, 0.f, 0.f};
#pragma unroll
  for (int i = 0; i < NH; ++i) {
    o[0] = fmaf(wi[i], g_v0(y[i][0], y[i][1]), o[0]); o[1] = fmaf(wi[i], g_v1(y[i][1]), o[1]);
    o[2] = fmaf(wi[i], g_v0(y[i][2], y[i][3]), o[2]); o[3] = fmaf(wi[i], g_v1(y[i][3]), o[3]);
  }
  float x = swadd16(swadd32(o[0], o[2]), swadd32(o[1], o[3]));
  x += dppf<ROR8>(x);
  if (c < 8) red[w * 32 + c * 4 + r] = x;
  __syncthreads();
  if (tid < 16) {
    f32x2 s2 = *(const f32x2*)(red + 2 * tid);
#pragma unroll
    for (int i = 1; i < 8; ++i) s2 += *(const f32x2*)(red + i * 32 + 2 * tid);
    *(unsigned*)(out + 2 * tid) = cvtpk(s2[0], s2[1]);
  }
}

__global__ void __launch_bounds__(512, 2)
attn_main(const unsigned short* Ax, const unsigned short* Ak, const unsigned short* Av, const unsigned short* Awq, const unsigned short* Awo,
          const unsigned short* Bx, const unsigned short* Bk, const unsigned short* Bv, const unsigned short* Bwq, const unsigned short* Bwo, float* ws, unsigned short* out) {
  __shared__ __attribute__((aligned(16))) float lds[L_TOTAL];
  const int bid = blockIdx.x, tid = threadIdx.x, lane = tid & 63;
  const int w = __builtin_amdgcn_readfirstlane(tid >> 6);
  const int r = lane >> 4, c = lane & 15;
  const int xcd = bid & 7, j = bid >> 3;
  const int b = w >> 2, wb = w & 3;
  const int vo_kv = lane * 16;
  const int vo_wq = r * (16 * 8192) + c * 16;
  const int vo_wo = (lane >> 3) * (16 * 1024) + (lane & 7) * 16;
  unsigned* opk = (unsigned*)(lds + L_OPK);
  unsigned cpf;
  {
    unsigned long long pc; int len;
    asm volatile("s_getpc_b64 %0\n.Lpf_base%=:\n\ts_mov_b32 %1, .Lpf_end-.Lpf_base%=" : "=s"(pc), "=s"(len));
    const rsrc_t rc = mkrs((const void*)pc, len > 0 ? len : 0);
    cpf = __builtin_amdgcn_raw_buffer_load_b32(rc, w == 7 ? lane * 128 : OOB, ((j + w) & 7) * 8192, 0);
    PIN();
  }
  const unsigned long long t_start = __builtin_amdgcn_s_memrealtime();
  const bool isB = j < 4 * NSB;
  const int h = 4 * xcd + j / NSB, kvh = xcd ^ 1;
  const rsrc_t rwq = mkrs(isB ? Bwq : Awq, 512 * 4096 * 2);
  const int sq = w * (64 * 8192) + (isB ? h * 256 : kvh * 1024);
  float* qdst = lds + L_QP + w * 256 + (r >> 1) * 128 + 8 * c + 4 * (r & 1);
  u32x4 xa[2];
  unsigned wm0;
  {
    u32x4 Wq[16];
    const rsrc_t rx = mkrs(isB ? Bx : Ax, 2048);
    xa[0] = bld<0>(rx, (c & 1) * 1024 + r * 32, w * 128); xa[1] = bld<0>(rx, (c & 1) * 1024 + r * 32 + 16, w * 128);
    wq_issue<AUX_W>(Wq, rwq, vo_wq, sq);
    PIN();
    {
      const int k = w >> 1, e = j * 2 + (w & 1);
      const rsrc_t rw = mkrs(k == 0 ? Bwo : (k == 1 ? Awo : Awq), k < 3 ? 512 * 4096 * 2 : 0);
      wm0 = __builtin_amdgcn_raw_buffer_load_b32(rw, k == 2 ? (lane >> 3) * 8192 + (lane & 7) * 128 : lane * 128,
                                                 k == 2 ? e * 65536 + kvh * 1024 : (k == 0 ? xcd : kvh) * 524288 + e * 8192, 0);
      PIN();
    }
    *(f32x4*)qdst = qproj(Wq, xa, r);
    asm volatile("" :: "v"(cpf));
    PIN();
  }
  const rsrc_t rws = mkrs(ws, WS_END * 8);
  u32x2 tag;
#define TAG_LOAD() tag = __builtin_amdgcn_raw_buffer_load_b64(rws, WS_EPOCH * 8, 0, SC1)
#define TAG_MAKE() do { asm volatile("" : "+v"(tag)); tag[0] += 1u; tag[1] += 1u; } while (0)

  if (isB) {
    constexpr int NSW = 7, G = NSB * NSW, TB = 16 * 256;
    constexpr int NFULL = SK / (16 * G), REST = (SK - NFULL * 16 * G) / 4;
    constexpr int NT = NFULL + 1, IPP = NT / 3, NIT = 4 * IPP, PT = PT_B;
    static_assert(NT % 3 == 0 && REST >= 3 * G && REST <= 4 * G, "three slots; only the last 4 keys of the partial tile can be missing");
    const int s = j % NSB, g = s * NSW + w;
    const int r0 = (g * REST) / G, r1 = ((g + 1) * REST) / G;
    const int last_off = (NFULL * 16 * G + r0 * 4) * 256, nrec = w < NSW ? (NFULL * 16 * G + r1 * 4) * 256 : 0;
    const bool short_last = r1 - r0 < 4;
    const rsrc_t rwo = mkrs(Bwo, 512 * 4096 * 2);
    const size_t kv0 = (size_t)h * SK * 128, kv1 = (size_t)(32 + h) * SK * 128;
    u32x4 W[16], F[16], T[3][4];
    auto desc = [&](auto ph) INL {
      constexpr int P = decltype(ph)::value;
      constexpr bool ISV = P >> 1, B1 = P & 1;
      return mkrs((ISV ? Bv : Bk) + (B1 ? kv1 : kv0), P < 4 ? nrec : 0);
    };
    auto issue = [&](u32x4 (&t)[4], rsrc_t rs, int n) INL {
      const int so = n == PT ? last_off : (g + (n > PT ? n - 1 : n) * G) * TB;
#pragma unroll
      for (int i = 0; i < 4; ++i) t[i] = bld<AUX_KV>(rs, vo_kv + i * 1024, so);
      PIN();
    };
    issue(T[0], desc(IC<0>{}), 0);
    issue(T[1], desc(IC<0>{}), 1);
    __syncthreads();
    OprojAddr oad;
    unsigned short* sco = (unsigned short*)(lds + L_SCO);
    if (w < NSW) {
      float acc[8];
      auto phase = [&](auto ph) INL {
        constexpr int P = decltype(ph)::value;
        constexpr bool ISV = P >> 1;
        constexpr int BB = P & 1;
        const rsrc_t rc = desc(ph), rn = desc(IC<P + 1>{});
        unsigned short* sp = sco + (w * 2 + BB) * NT * 16 + r * 4;
        float mx = M_INIT;
        u32x4 q;
        if (!ISV) q = q_gather(lds + L_QP, BB, r, c);
        if (ISV) {
#pragma unroll
          for (int k = 0; k < 8; ++k) acc[k] = 0.f;
        }
        auto tile = [&](auto slot, auto tail, int n) INL {
          constexpr int SL = decltype(slot)::value, TAIL = decltype(tail)::value;
          u32x4 (&t)[4] = T[SL];
          u32x2 pp;
          if (ISV) pp = *(const u32x2*)(sp + n * 16);
          if constexpr (!(TAIL && P == 3)) {
#pragma unroll
            for (int i = 0; i < 4; ++i) asm volatile("" : "+v"(t[i]) :: "memory");
            __builtin_amdgcn_sched_barrier(0);
            issue(T[(SL + 2) % 3], TAIL ? rn : rc, TAIL ? n + 2 - NT : n + 2);
          }
          if (!ISV) {
            float x[4];
#pragma unroll
            for (int i = 0; i < 4; ++i) {
              float d = dot2(t[i][0], q[0], 0.f);
              d = dot2(t[i][1], q[1], d); d = dot2(t[i][2], q[2], d); x[i] = dot2(t[i][3], q[3], d);
            }
            const float a0 = x[0] + dppf<ROR8>(x[0]), a1 = x[1] + dppf<ROR8>(x[1]), a2 = x[2] + dppf<ROR8>(x[2]), a3 = x[3] + dppf<ROR8>(x[3]);
            const float b0 = (c & 8) ? a2 : a0, b1 = (c & 8) ? a3 : a1;
            const float c0 = b0 + dppf<XOR7>(b0), c1 = b1 + dppf<XOR7>(b1);
            float sc = (c & 4) ? c1 : c0;
            sc += dppf<XOR2>(sc); sc += dppf<XOR1>(sc);
            sc = __uint_as_float(cvtpk(sc, sc) << 16) * QK_SCALE;
            if (n == PT && short_last && c >= 12) sc = -INFINITY;
            mx = fmaxf(mx, sc);
            if ((c & 3) == 0) sp[n * 16 + (c >> 2)] = (unsigned short)cvtpk(sc, sc);
            asm volatile("" : "+v"(mx) :: "memory");
          } else {
#pragma unroll
            for (int i = 0; i < 2; ++i)
#pragma unroll
              for (int jj = 0; jj < 4; ++jj) {
                acc[2 * jj] = dot2(pk_lo(t[2 * i][jj], t[2 * i + 1][jj]), pp[i], acc[2 * jj]);
                acc[2 * jj + 1] = dot2(pk_hi(t[2 * i][jj], t[2 * i + 1][jj]), pp[i], acc[2 * jj + 1]);
              }
#pragma unroll
            for (int k = 0; k < 8; ++k) asm volatile("" : "+v"(acc[k]) :: "memory");
          }
          __builtin_amdgcn_sched_barrier(0);
        };
        if constexpr (P == 0) {
          static_for<NT - 3>([&](auto nn) INL {
            constexpr int N = decltype(nn)::value;
            if constexpr (N >= 2 && N <= 17) { W[N - 2] = bld<AUX_W>(rwo, vo_wo, h * (128 * 1024) + w * 128 + (N - 2) * 1024); PIN(); }
            tile(IC<N % 3>{}, IC<0>{}, N);
            BAR();
          });
        } else {
          for (int it = 0; it < IPP - 1; ++it) {
            tile(IC<0>{}, IC<0>{}, 3 * it); BAR();
            tile(IC<1>{}, IC<0>{}, 3 * it + 1); BAR();
            tile(IC<2>{}, IC<0>{}, 3 * it + 2); BAR();
          }
        }
        tile(IC<0>{}, IC<0>{}, NT - 3); BAR();
        tile(IC<1>{}, IC<1>{}, NT - 2); BAR();
        tile(IC<2>{}, IC<1>{}, NT - 1);
        if (!ISV) {
          mx = fmaxf(mx, dppf<ROR8>(mx)); mx = fmaxf(mx, dppf<XOR7>(mx)); mx = xrow_max(mx);
          if (lane == 0) lds[L_ML + w * 8 + BB * 2] = __uint_as_float(cvtpk(mx, mx) << 16);
        } else {
          float t4[4];
#pragma unroll
          for (int i = 0; i < 4; ++i) t4[i] = swadd32(acc[i], acc[i + 4]);
          f32x2 u2; u2[0] = swadd16(t4[0], t4[2]); u2[1] = swadd16(t4[1], t4[3]);
          *(f32x2*)(lds + L_REC + w * 512 + BB * 128 + 8 * c + 4 * (r >> 1) + 2 * (r & 1)) = u2;
        }
        asm volatile("s_waitcnt lgkmcnt(0)" ::: "memory");
        BAR();
        if (!ISV) {
          float M = lds[L_ML + BB * 2];
#pragma unroll
          for (int i = 1; i < NSW; ++i) M = fmaxf(M, lds[L_ML + i * 8 + BB * 2]);
          float l = 0.f;
          u32x2* cp = (u32x2*)(sco + (w * 2 + BB) * NT * 16);
#pragma unroll
          for (int k = 0; k < 2; ++k) {
            const int idx = lane + 64 * k;
            if (idx < NT * 4) {
              const u32x2 v = cp[idx];
              const float p0 = ex2((__uint_as_float(v[0] << 16) - M) * LOG2E), p1 = ex2((__uint_as_float(v[0] & 0xffff0000u) - M) * LOG2E);
              const float p2 = ex2((__uint_as_float(v[1] << 16) - M) * LOG2E), p3 = ex2((__uint_as_float(v[1] & 0xffff0000u) - M) * LOG2E);
              l += (p0 + p1) + (p2 + p3);
              cp[idx] = u32x2{cvtpk(p0, p1), cvtpk(p2, p3)};
            }
          }
          l = xrow_sum(row_sum(l));
          if (lane == 0) lds[L_ML + w * 8 + BB * 2 + 1] = l;
          asm volatile("s_waitcnt lgkmcnt(0)" ::: "memory");
          PIN();
        }
      };
      phase(IC<0>{});
      asm volatile("" :: "v"(wm0));
      phase(IC<1>{});
      phase(IC<2>{});
      to_frags(W, F);
#pragma unroll
      for (int i = 0; i < 16; ++i) asm volatile("" : "+v"(F[i]));
      oad = oproj_addr(NSB, *(const u32x2*)(lds + L_TAG), w, lane);
      PIN();
      phase(IC<3>{});
    } else {
      wo_issue<AUX_W>(W, rwo, vo_wo, h * (128 * 1024) + w * 128);
      TAG_LOAD();
      const unsigned tb = (unsigned)t_start;
      unsigned row = 0u;
      float mu1 = 0.f, mu2 = 0.f, mu3 = 0.f;
      int tgt = 0, stp = 0, cap = 0, prev = 0;
      auto hold = [&]() INL {
        const int t = min(tgt, prev + cap);
        while ((int)((unsigned)__builtin_amdgcn_s_memrealtime() - tb) < t) __builtin_amdgcn_s_sleep(1);
        __builtin_amdgcn_s_barrier();
        prev = (int)((unsigned)__builtin_amdgcn_s_memrealtime() - tb);
        tgt += stp;
      };
      auto sum_o = [&](int bs) INL {
        f32x2 o = *(const f32x2*)(lds + L_REC + bs * 128 + 2 * lane);
#pragma unroll
        for (int i = 1; i < NSW; ++i) o += *(const f32x2*)(lds + L_REC + i * 512 + bs * 128 + 2 * lane);
        opk[bs * 256 + lane] = cvtpk(o[0], o[1]);
      };
      const bool pub = s == 0 && (h & (32 / NPUB - 1)) == 0;
      const char* ttab = (const char*)ws + WS_TT * 8;
      int ownv = 0, req = -1, young = 0, i1 = 0, i2 = 0, i3 = 0, got = 0;
      auto step = [&](int it) INL {
        hold(); hold(); hold();
        ownv = lane == it ? prev : ownv;
        if (req >= 0 && (int)__builtin_amdgcn_s_getreg(7 | (3 << 11)) <= young) {
          asm volatile("" : "+v"(row) :: "memory");
          float x = (float)(int)((tb + prev - row < 3000u ? row : tb + prev) - tb);
          x = row_sum(x);
          mu3 = mu2; mu2 = mu1; mu1 = swadd16(x, x) * (1.f / 32.f);
          i3 = i2; i2 = i1; i1 = req; req = -1;
          if (++got >= 3) {
            const float per = fminf((mu1 - mu3) / (float)(i1 - i3), 600.f);
            const bool lead = __builtin_amdgcn_readfirstlane((int)mu1) > __builtin_amdgcn_readlane(ownv, i1);
            tgt = lead ? __builtin_amdgcn_readfirstlane((int)(mu1 + ((float)(it - i1) + 1.f / 3.f) * per)) - MARGIN : 0;
            stp = lead ? __builtin_amdgcn_readfirstlane((int)(per * (1.f / 3.f))) : 0;
            cap = __builtin_amdgcn_readfirstlane((int)(per * ((1.f + PCAP * 0.01f) / 3.f)));
          }
        }
        if (it >= NIT - PSTOP) { tgt = 0; stp = 0; }
        if (req < 0 && it >= PSTART - 3 && it < NIT - PSTOP - 1) {
          req = it - RLAG; young = 0;
          asm volatile("global_load_dword %0, %1, %2 sc1" : "+v"(row) : "v"(req * 4096 + (lane & (NPUB - 1)) * 128), "s"(ttab) : "memory");
        }
        if (pub && it >= PSTART - 3 - RLAG && it < NIT - PSTOP - 1 - RLAG) {
          ++young;
          __builtin_amdgcn_raw_buffer_store_b32(tb + prev, rws, lane < 32 ? WS_TT * 8 + it * 4096 + (h / (32 / NPUB)) * 128 + lane * 4 : OOB, 0, SC1);
        }
        PIN();
      };
      step(0);
      asm volatile("" :: "v"(wm0));
      to_frags(W, F);
#pragma unroll
      for (int i = 0; i < 16; ++i) asm volatile("" : "+v"(F[i]));
      TAG_MAKE();
      if (lane == 0) *(u32x2*)(lds + L_TAG) = tag;
      asm volatile("s_waitcnt lgkmcnt(0)" ::: "memory");
      oad = oproj_addr(NSB, tag, w, lane);
#pragma unroll 1
      for (int it = 1; it < 2 * IPP + 1; ++it) step(it);
      asm volatile("" ::: "memory");
      if (lane < 2) {
        float M = lds[L_ML + lane * 2], L = lds[L_ML + lane * 2 + 1];
#pragma unroll
        for (int i = 1; i < NSW; ++i) { M = fmaxf(M, lds[L_ML + i * 8 + lane * 2]); L += lds[L_ML + i * 8 + lane * 2 + 1]; }
        st_ml(rws, WS_MLB + ((lane * 32 + h) * 8 + s) * 2, M, L, tag);
      }
#pragma unroll 1
      for (int it = 2 * IPP + 1; it < 3 * IPP + 1; ++it) step(it);
      asm volatile("" ::: "memory");
      sum_o(0);
#pragma unroll 1
      for (int it = 3 * IPP + 1; it < NIT - 1; ++it) step(it);
      asm volatile("s_waitcnt vmcnt(0)" : "+v"(row) :: "memory");
      hold(); hold();
      __builtin_amdgcn_s_barrier();
      asm volatile("" ::: "memory");
      sum_o(1);
    }
    __syncthreads();
    oproj_store(F, opk, rws, WS_YB + (h * NSB + s) * 16, oad, lane);
    asm volatile(".Lpf_end:");
  } else {
    constexpr int TB = 16 * 256;
    const int qt = j - 4 * NSB;
    const rsrc_t rwo = mkrs(Awo, 512 * 4096 * 2);
    const size_t kvbase = ((size_t)(b * 8 + kvh) * SK + (SK - WIN) + qt * (WIN / NQA)) * 128;
    const rsrc_t rk = mkrs(Ak + kvbase, (WIN / NQA) * 256), rv = mkrs(Av + kvbase, (WIN / NQA) * 256);
    u32x4 W0[16], W1[16], K[4], V[4];
    const int ya = WS_YA + (kvh * 4 * NQA + qt) * 16;
    wq_issue<AUX_W>(W1, rwq, vo_wq, sq + 256);
    PIN();
    const int fa = qt * 8 + xcd, fb = fa >> 4, fc = fa & 15;
    TAG_LOAD();
    PIN();
    wq_issue<AUX_W>(W0, rwq, vo_wq, sq + 512);
    PIN();
    *(f32x4*)(qdst + 2048) = qproj(W1, xa, r);
    PIN();
    wq_issue<AUX_W>(W1, rwq, vo_wq, sq + 768);
    PIN();
    int so = wb * TB;
#pragma unroll
    for (int i = 0; i < 4; ++i) K[i] = bld<AUX_KV>(rk, vo_kv + i * 1024, so);
    PIN();
#pragma unroll
    for (int i = 0; i < 4; ++i) V[i] = bld<AUX_KV>(rv, vo_kv + i * 1024, so);
    PIN();
    *(f32x4*)(qdst + 4096) = qproj(W0, xa, r);
    PIN();
    *(f32x4*)(qdst + 6144) = qproj(W1, xa, r);
    PIN();
    const int swo = kvh * (512 * 1024) + w * 128;
    wo_issue<AUX_W>(W0, rwo, vo_wo, swo);
    PIN();
    __syncthreads();
    TAG_MAKE();
    asm volatile("" :: "v"(wm0));
    u32x4 q[4];
#pragma unroll
    for (int hh = 0; hh < 4; ++hh) q[hh] = q_gather(lds + L_QP + hh * 2048, b, r, c);
    float m = M_INIT, l = 0.f, acc[32];
#pragma unroll
    for (int k = 0; k < 32; ++k) acc[k] = 0.f;
    constexpr int NTA = WIN / NQA / 16 / 4;
    for (int t = 0; t < NTA; ++t) {
      so += 4 * TB;
      float x[16];
#pragma unroll
      for (int i = 0; i < 4; ++i)
#pragma unroll
        for (int hh = 0; hh < 4; ++hh) {
          float d = dot2(K[i][0], q[hh][0], 0.f);
          d = dot2(K[i][1], q[hh][1], d); d = dot2(K[i][2], q[hh][2], d); x[i * 4 + hh] = dot2(K[i][3], q[hh][3], d);
        }
#pragma unroll
      for (int i = 0; i < 16; ++i) asm volatile("" : "+v"(x[i]) :: "memory");
      __builtin_amdgcn_sched_barrier(0);
#pragma unroll
      for (int i = 0; i < 4; ++i) K[i] = bld<AUX_KV>(rk, vo_kv + i * 1024, so);
      PIN();
#define TR_STEP(HALF, CTRL, BIT) _Pragma("unroll") for (int hh = 0; hh < (HALF); ++hh) { \
        const float lo_ = x[hh], hi_ = x[hh + (HALF)]; const float t1_ = lo_ + dppf<CTRL>(lo_), t2_ = hi_ + dppf<CTRL>(hi_); \
        x[hh] = (c & (BIT)) ? t2_ : t1_; }
      TR_STEP(8, ROR8, 8) TR_STEP(4, XOR7, 4) TR_STEP(2, XOR2, 2) TR_STEP(1, XOR1, 1)
#undef TR_STEP
      float sc = x[0], sc_dummy = x[0];
      round_scores(sc, sc_dummy);
      float mx = fmaxf(sc, dppf<ROR4>(sc)); mx = fmaxf(mx, dppf<ROR8>(mx));
      if (__any(mx > m)) {
        const float mn = fmaxf(m, mx), al = ex2((m - mn) * LOG2E);
        m = mn; l *= al;
        const float a0 = dppf<BCAST + 0>(al), a1 = dppf<BCAST + 1>(al), a2 = dppf<BCAST + 2>(al), a3 = dppf<BCAST + 3>(al);
#pragma unroll
        for (int k = 0; k < 8; ++k) { acc[k] *= a0; acc[8 + k] *= a1; acc[16 + k] *= a2; acc[24 + k] *= a3; }
      }
      const float p = ex2((sc - m) * LOG2E);
      l += p;
      const unsigned pp = cvtpk(p, dppf<ROR12>(p));
      unsigned pb[8];
      pb[0] = dppu<BCAST + 0>(pp); pb[1] = dppu<BCAST + 1>(pp); pb[2] = dppu<BCAST + 2>(pp); pb[3] = dppu<BCAST + 3>(pp);
      pb[4] = dppu<BCAST + 8>(pp); pb[5] = dppu<BCAST + 9>(pp); pb[6] = dppu<BCAST + 10>(pp); pb[7] = dppu<BCAST + 11>(pp);
#pragma unroll
      for (int i = 0; i < 2; ++i)
#pragma unroll
        for (int jj = 0; jj < 4; ++jj) {
          const unsigned lo = pk_lo(V[2 * i][jj], V[2 * i + 1][jj]), hi = pk_hi(V[2 * i][jj], V[2 * i + 1][jj]);
#pragma unroll
          for (int hh = 0; hh < 4; ++hh) {
            acc[hh * 8 + 2 * jj] = dot2(lo, pb[i * 4 + hh], acc[hh * 8 + 2 * jj]);
            acc[hh * 8 + 2 * jj + 1] = dot2(hi, pb[i * 4 + hh], acc[hh * 8 + 2 * jj + 1]);
          }
        }
#pragma unroll
      for (int k = 0; k < 32; ++k) asm volatile("" : "+v"(acc[k]) :: "memory");
      __builtin_amdgcn_sched_barrier(0);
#pragma unroll
      for (int i = 0; i < 4; ++i) V[i] = bld<AUX_KV>(rv, vo_kv + i * 1024, so);
      PIN();
      BAR();
    }
    wo_issue<AUX_W>(W1, rwo, vo_wo, swo + 128 * 1024);
    PIN();
    {
      const float mm = xrow_max(m), sc = ex2((m - mm) * LOG2E);
      float ls = l * sc; ls += dppf<ROR4>(ls); ls += dppf<ROR8>(ls); ls = xrow_sum(ls);
      float sh[4];
      sh[0] = dppf<BCAST + 0>(sc); sh[1] = dppf<BCAST + 1>(sc); sh[2] = dppf<BCAST + 2>(sc); sh[3] = dppf<BCAST + 3>(sc);
#pragma unroll
      for (int hh = 0; hh < 4; ++hh) {
        float t[4];
#pragma unroll
        for (int i = 0; i < 4; ++i) t[i] = swadd32(acc[hh * 8 + i] * sh[hh], acc[hh * 8 + i + 4] * sh[hh]);
        f32x2 u; u[0] = swadd16(t[0], t[2]); u[1] = swadd16(t[1], t[3]);
        *(f32x2*)(lds + L_REC + w * 512 + hh * 128 + 8 * c + 4 * (r >> 1) + 2 * (r & 1)) = u;
      }
      if (lane < 4) *(f32x2*)(lds + L_ML + w * 8 + lane * 2) = f32x2{mm, ls};
    }
    __syncthreads();
    {
      const int w0 = b * 4, hh = wb;
      f32x2 ml[4];
#pragma unroll
      for (int i = 0; i < 4; ++i) ml[i] = *(const f32x2*)(lds + L_ML + (w0 + i) * 8 + hh * 2);
      const float M = fmaxf(fmaxf(ml[0][0], ml[1][0]), fmaxf(ml[2][0], ml[3][0]));
      float L = 0.f, o0 = 0.f, o1 = 0.f;
#pragma unroll
      for (int i = 0; i < 4; ++i) {
        const float e = ex2((ml[i][0] - M) * LOG2E);
        const f32x2 v = *(const f32x2*)(lds + L_REC + (w0 + i) * 512 + hh * 128 + 2 * lane);
        L = fmaf(e, ml[i][1], L); o0 = fmaf(e, v[0], o0); o1 = fmaf(e, v[1], o1);
      }
      opk[b * 256 + hh * 64 + lane] = cvtpk(o0, o1);
      if (lane == 0) st_ml(rws, WS_MLA + ((b * 32 + kvh * 4 + hh) * 8 + qt) * 2, M, L, tag);
    }
    __syncthreads();
    u32x4 F[16];
    const OprojAddr oad = oproj_addr(NQA, tag, w, lane);
    to_frags(W0, F); oproj_store(F, opk, rws, ya, oad, lane);
    PIN();
    wo_issue<AUX_W>(W0, rwo, vo_wo, swo + 2 * 128 * 1024);
    PIN();
    to_frags(W1, F); oproj_store(F, opk + 64, rws, ya + NQA * 16, oad, lane);
    PIN();
    wo_issue<AUX_W>(W1, rwo, vo_wo, swo + 3 * 128 * 1024);
    PIN();
    to_frags(W0, F); oproj_store(F, opk + 128, rws, ya + 2 * NQA * 16, oad, lane);
    PIN();
    to_frags(W1, F); oproj_store(F, opk + 192, rws, ya + 3 * NQA * 16, oad, lane);
    {
      const unsigned long long t_lim = t_start + T_LIMIT;
      final_merge<NQA>(rws, WS_YA + fa * 32 * NQA * 16, WS_MLA + fb * 32 * 16, tag, lds + L_RED, out + fb * 1024 + fc * 32, tid, w, lane, t_lim);
      final_merge<NSB>(rws, WS_YB + fa * 32 * NSB * 16, WS_MLB + fb * 32 * 16, tag, lds + L_RED + 256, out + fb * 1024 + 512 + fc * 32, tid, w, lane, t_lim);
      if (fa == 0 && tid == 0) __builtin_amdgcn_raw_buffer_store_b64(tag, rws, WS_EPOCH * 8, 0, SC1);
    }
  }
}

extern "C" void kernel_launch(void* const* d_in, const int* in_sizes, int n_in, void* d_out, int out_size, void* d_ws, size_t ws_size,
                              hipStream_t stream) {
  typedef const unsigned short* cp;
  attn_main<<<256, 512, 0, stream>>>((cp)d_in[0], (cp)d_in[1], (cp)d_in[2], (cp)d_in[3], (cp)d_in[4], (cp)d_in[5], (cp)d_in[6], (cp)d_in[7],
                                     (cp)d_in[8], (cp)d_in[9], (float*)d_ws, (unsigned short*)d_out);
}
```
